```python
import torch

def _softmax(x, axis=-1):
    return torch.softmax(x, dim=axis)
def _gelu(x):
    return torch.nn.functional.gelu(x, approximate="tanh")
def _silu(x):
    return torch.nn.functional.silu(x)
def _sigmoid(x):
    return torch.sigmoid(x)
def _layernorm(x, axis=-1, eps=1e-5):
    m = x.mean(dim=axis, keepdim=True)
    v = x.var(dim=axis, keepdim=True, unbiased=False)
    return (x - m) / torch.sqrt(v + eps)
def _scatter_add(tgt, idx, src, axis=0):
    return tgt.clone().index_add_(axis, idx, src)
def _segment_sum(src, seg, n):
    out = torch.zeros((n,) + src.shape[1:], dtype=src.dtype)
    return out.index_add_(0, seg, src)
def _pad(x, pw):
    flat = tuple(v for pair in reversed(pw) for v in pair)
    return torch.nn.functional.pad(x, flat)

kernel_name = 'concat_heads__spectral_gfnet__mha_block__prompt'

def setup_inputs():

    _w = (lambda a: (lambda t: t.to(torch.bfloat16) if t.is_floating_point() else t)(torch.as_tensor(a)))
    def _A_setup():
        B, S, D = 1, 2048, 448
        Df = D // 2 + 1
        return {
            "x": _w((lambda *s: torch.randn(*s, dtype=torch.bfloat16) * 0.05)(B, S, D)),
            "w_r": _w((lambda *s: torch.randn(*s, dtype=torch.bfloat16) * 6.4)(S, Df) * 0.02),
            "w_i": _w((lambda *s: torch.randn(*s, dtype=torch.bfloat16) * 6.4)(S, Df) * 0.02),
        }
    _A_in = _A_setup()

    def _B_setup():
        B, S, D, H = 1, 2048, 448, 16
        return {
            "x": _w((lambda *s: torch.randn(*s, dtype=torch.bfloat16) * 0.2)(B, S, D)),
            "wqkv": _w((lambda *s: torch.randn(*s, dtype=torch.bfloat16) * 0.2)(D, 3 * D)),
            "wo": _w((lambda *s: torch.randn(*s, dtype=torch.bfloat16) * 0.05)(D, D)),
        }
    _B_in = _B_setup()

    return {
        "A_x": _A_in["x"],
        "A_w_r": _A_in["w_r"],
        "A_w_i": _A_in["w_i"],
        "B_x": _B_in["x"],
        "B_wqkv": _B_in["wqkv"],
        "B_wo": _B_in["wo"],
    }

def reference(A_x, A_w_r, A_w_i, B_x, B_wqkv, B_wo):
    def _A_ref(x, w_r, w_i):
        B, S, D = x.shape
        X = (lambda x, axes=(-2,-1): torch.fft.rfft2(x.float(), dim=axes))(x, axes=(-2, -1))
        W = w_r + 1j * w_i
        X = X * W
        y = (lambda x, s, axes=(-2,-1): torch.fft.irfft2(x, s=s, dim=axes).to(torch.bfloat16))(X, (S, D), axes=(-2, -1))
        return y

    def _B_ref(x, wqkv, wo):
        B, S, D = x.shape
        H = 16
        Dh = D // H
        qkv = (lambda x, *d: x.permute(*d))((x @ wqkv).reshape(B, S, 3, H, Dh), 2, 0, 3, 1, 4)
        q, k, v = qkv[0], qkv[1], qkv[2]
        scores = torch.einsum("bhqd,bhkd->bhqk", q, k) * (Dh ** -0.5)
        p = _softmax(scores, axis=-1)
        o = torch.einsum("bhqk,bhkd->bhqd", p, v)
        return (lambda x, *d: x.permute(*d))(o, 0, 2, 1, 3).reshape(B, S, D) @ wo

    a = _A_ref(A_x, A_w_r, A_w_i)
    b = _B_ref(B_x, B_wqkv, B_wo)
    return torch.concatenate([a, b], axis=-1)
```

```cpp
#include <hip/hip_runtime.h>
#include <hip/hip_bf16.h>
#include <stdint.h>
typedef unsigned short u16;
__device__ inline float bf2f(u16 v){ return __uint_as_float((unsigned)v<<16); }
__device__ inline u16 f2bf(float f){ unsigned u=__float_as_uint(f); u += 0x7fffu + ((u>>16)&1u); return (u16)(u>>16); }
constexpr int S=2048,D=448,DF=225,H=16,DH=28;
__global__ void a1(const u16* x, float2* X1){
  int k=blockIdx.x*blockDim.x+threadIdx.x, s=blockIdx.y; if(k>=DF) return;
  float re=0,im=0;
  for(int d=0;d<D;++d){ int r=(d*k)%D; float sn,cs; sincospif(2.0f*r/D,&sn,&cs); float v=bf2f(x[s*D+d]); re+=v*cs; im-=v*sn; }
  X1[s*DF+k]=make_float2(re,im);
}
__global__ void a2(const float2* X1,const u16* wr,const u16* wi,float2* X2,int inv){
  __shared__ float2 tw[S];
  for(int i=threadIdx.x;i<S;i+=blockDim.x){ float sn,cs; sincospif(2.0f*i/S,&sn,&cs); tw[i]=make_float2(cs,inv?sn:-sn);}
  __syncthreads();
  int k=blockIdx.x*blockDim.x+threadIdx.x, f=blockIdx.y; if(k>=DF) return;
  float re=0,im=0;
  for(int s=0;s<S;++s){ float2 t=tw[(s*f)&(S-1)]; float2 v=X1[s*DF+k]; re+=v.x*t.x-v.y*t.y; im+=v.x*t.y+v.y*t.x; }
  if(!inv){ float a=bf2f(wr[f*DF+k]), b=bf2f(wi[f*DF+k]); float r2=re*a-im*b, i2=re*b+im*a; re=r2; im=i2; }
  X2[f*DF+k]=make_float2(re,im);
}
__global__ void a3(const float2* Z,u16* out){
  int n=blockIdx.x*blockDim.x+threadIdx.x, s=blockIdx.y; if(n>=D) return;
  float acc=0;
  for(int k=0;k<DF;++k){ int r=(n*k)%D; float sn,cs; sincospif(2.0f*r/D,&sn,&cs); float2 v=Z[s*DF+k]; float c=(k==0||k==DF-1)?1.f:2.f;
    if(k==0||k==DF-1) acc+=v.x*cs; else acc+=c*(v.x*cs-v.y*sn); }
  out[s*2*D+n]=f2bf(acc/(float)(S*D));
}
__global__ void gemm(const u16* a,const u16* b,u16* c,int M,int N,int K,int ldc){
  int n=blockIdx.x*blockDim.x+threadIdx.x, m=blockIdx.y; if(n>=N) return;
  float acc=0; for(int k=0;k<K;++k) acc+=bf2f(a[m*K+k])*bf2f(b[k*N+n]);
  c[m*ldc+n]=f2bf(acc);
}
__global__ void attn(const u16* qkv,u16* o){
  __shared__ float p[S]; __shared__ float red[256];
  int q=blockIdx.x,h=blockIdx.y,t=threadIdx.x;
  float qv[DH]; for(int d=0;d<DH;++d) qv[d]=bf2f(qkv[q*3*D+h*DH+d]);
  float mx=-1e30f;
  for(int k=t;k<S;k+=256){ float s=0; for(int d=0;d<DH;++d) s+=qv[d]*bf2f(qkv[k*3*D+D+h*DH+d]); s*=0.1889822365f; p[k]=s; mx=fmaxf(mx,s);}
  red[t]=mx; __syncthreads(); for(int o2=128;o2>0;o2>>=1){ if(t<o2) red[t]=fmaxf(red[t],red[t+o2]); __syncthreads(); }
  mx=red[0]; __syncthreads();
  float sum=0; for(int k=t;k<S;k+=256){ float e=__expf(p[k]-mx); p[k]=e; sum+=e; }
  red[t]=sum; __syncthreads(); for(int o2=128;o2>0;o2>>=1){ if(t<o2) red[t]+=red[t+o2]; __syncthreads(); }
  sum=red[0]; __syncthreads();
  if(t<DH){ float acc=0; for(int k=0;k<S;++k) acc+=p[k]*bf2f(qkv[k*3*D+2*D+h*DH+t]); o[q*D+h*DH+t]=f2bf(acc/sum);}
}
extern "C" void kernel_launch(void* const* d_in, const int* in_sizes, int n_in, void* d_out, int out_size, void* d_ws, size_t ws_size, hipStream_t stream){
  const u16* ax=(const u16*)d_in[0]; const u16* wr=(const u16*)d_in[1]; const u16* wi=(const u16*)d_in[2];
  const u16* bx=(const u16*)d_in[3]; const u16* wqkv=(const u16*)d_in[4]; const u16* wo=(const u16*)d_in[5];
  u16* out=(u16*)d_out; char* ws=(char*)d_ws;
  float2* X1=(float2*)ws; float2* X2=(float2*)(ws+(8<<20)); u16* qkv=(u16*)(ws+(16<<20)); u16* o=(u16*)(ws+(32<<20));
  a1<<<dim3(1,S),256,0,stream>>>(ax,X1);
  a2<<<dim3(1,S),256,0,stream>>>(X1,wr,wi,X2,0);
  a2<<<dim3(1,S),256,0,stream>>>(X2,wr,wi,X1,1);
  a3<<<dim3(2,S),256,0,stream>>>(X1,out);
  gemm<<<dim3(6,S),256,0,stream>>>(bx,wqkv,qkv,S,3*D,D,3*D);
  attn<<<dim3(S,H),256,0,stream>>>(qkv,o);
  gemm<<<dim3(2,S),256,0,stream>>>(o,wo,out+D,S,D,D,2*D);
}
```

```cpp
#include <hip/hip_runtime.h>
#include <stdint.h>

typedef unsigned short u16;
typedef short bf16x8 __attribute__((ext_vector_type(8)));
typedef short s16x4 __attribute__((ext_vector_type(4)));
typedef float f32x16 __attribute__((ext_vector_type(16)));
typedef float f32x4 __attribute__((ext_vector_type(4)));
typedef float f32x2 __attribute__((ext_vector_type(2)));
typedef unsigned u32x4 __attribute__((ext_vector_type(4)));
typedef unsigned u32x2 __attribute__((ext_vector_type(2)));
typedef __bf16 bf16x2_t __attribute__((ext_vector_type(2)));
#define LDS_AS __attribute__((address_space(3)))
#define DI __device__ __forceinline__
#define MFMA(a, b, c) __builtin_amdgcn_mfma_f32_32x32x16_bf16((a), (b), (c), 0, 0, 0)
#define SBAR() __builtin_amdgcn_sched_barrier(0)
#define PIN(x) asm volatile("" : "+v"(x))

constexpr int SEQ = 2048, DM = 448, NH = 16, DF = 225;

namespace ct {
constexpr double PI = 3.14159265358979323846264338327950288;
constexpr double tsin(double x) { double x2 = x * x, t = x, s = x; for (int i = 1; i < 12; ++i) { t *= -x2 / ((2 * i) * (2 * i + 1)); s += t; } return s; }
constexpr double tcos(double x) { double x2 = x * x, t = 1, s = 1; for (int i = 1; i < 12; ++i) { t *= -x2 / ((2 * i - 1) * (2 * i)); s += t; } return s; }
constexpr double cos2pi(int j, int N) {
  j %= N; if (j < 0) j += N; if (2 * j > N) j = N - j; bool neg = false; if (4 * j > N) { j = N / 2 - j; neg = true; }
  double r = (8 * j > N) ? tsin(2 * PI * (N / 4 - j) / N) : tcos(2 * PI * j / N); return neg ? -r : r; }
constexpr double sin2pi(int j, int N) { return cos2pi(j - N / 4, N); }
constexpr u16 bf(double v) { float f = (float)v; unsigned u = __builtin_bit_cast(unsigned, f); u += 0x7fffu + ((u >> 16) & 1u); return (u16)(u >> 16); }
struct C448 { u16 c[448]; u16 ns[448]; };
constexpr C448 mk448() { C448 t{}; for (int j = 0; j < 448; ++j) { t.c[j] = bf(cos2pi(j, 448)); t.ns[j] = bf(-sin2pi(j, 448)); } return t; }
constexpr C448 T448 = mk448();
struct FPart { u16 v[56 * 448]; };
constexpr FPart mkF(int part) { FPart t{};
  for (int r = 0; r < 56; ++r) { int d = part * 56 + r; int a = 0; u16* o = &t.v[r * 448];
    for (int c = 0; c < 224; ++c) { o[2 * c] = T448.c[a]; o[2 * c + 1] = T448.ns[a]; a += d; if (a >= 448) a -= 448; }
    o[1] = (d & 1) ? 0xbf80 : 0x3f80; }
  return t; }
constexpr FPart F0 = mkF(0), F1 = mkF(1), F2 = mkF(2), F3 = mkF(3), F4 = mkF(4), F5 = mkF(5), F6 = mkF(6), F7 = mkF(7);
struct FTab { FPart p0, p1, p2, p3, p4, p5, p6, p7; u16 pad[128]; };
constexpr int kperm(int t, int h, int j) { return 16 * t + 8 * (j >> 2) + 4 * h + (j & 3); }
struct A2Tab {
  u16 t32f[3][2][64][8];
  u16 t64[3][2][4][64][8];
  u16 t32i[3][2][64][8];
};
constexpr A2Tab mkA2() { A2Tab t{};
  const double SC = 2.0 / (2048.0 * 448.0);
  for (int s = 0; s < 2; ++s) for (int l = 0; l < 64; ++l) for (int j = 0; j < 8; ++j) {
    int a = kperm(s, l >> 5, j) * (l & 31);
    t.t32f[0][s][l][j] = bf(cos2pi(a, 32)); t.t32f[1][s][l][j] = bf(sin2pi(a, 32)); t.t32f[2][s][l][j] = bf(-sin2pi(a, 32));
    int b = (16 * s + 8 * (l >> 5) + j) * (l & 31);
    t.t32i[0][s][l][j] = bf(SC * cos2pi(b, 32)); t.t32i[1][s][l][j] = bf(SC * sin2pi(b, 32)); t.t32i[2][s][l][j] = bf(-SC * sin2pi(b, 32)); }
  for (int m = 0; m < 2; ++m) for (int k = 0; k < 4; ++k) for (int l = 0; l < 64; ++l) for (int j = 0; j < 8; ++j) {
    int a = (32 * m + (l & 31)) * (32 * (k >> 1) + kperm(k & 1, l >> 5, j));
    t.t64[0][m][k][l][j] = bf(cos2pi(a, 64)); t.t64[1][m][k][l][j] = bf(sin2pi(a, 64)); t.t64[2][m][k][l][j] = bf(-sin2pi(a, 64)); }
  return t; }
struct TwTab { float tw[2][4][2][64][4]; };
constexpr TwTab mkTw() { TwTab t{};
  for (int m = 0; m < 2; ++m) for (int g = 0; g < 4; ++g) for (int l = 0; l < 64; ++l) for (int i = 0; i < 4; ++i) {
    int a = (32 * m + 8 * g + 4 * (l >> 5) + i) * (l & 31);
    t.tw[m][g][0][l][i] = (float)cos2pi(a, 2048); t.tw[m][g][1][l][i] = (float)sin2pi(a, 2048); }
  return t; }
}
__device__ __attribute__((aligned(256))) const ct::FTab g_F = { ct::F0, ct::F1, ct::F2, ct::F3, ct::F4, ct::F5, ct::F6, ct::F7, {} };
__device__ __attribute__((aligned(256))) const ct::A2Tab g_A2 = ct::mkA2();
__device__ __attribute__((aligned(256))) const ct::TwTab g_TW = ct::mkTw();

constexpr size_t WS_QF = 0;
constexpr size_t WS_KV = WS_QF + (2u << 20);
constexpr size_t WS_X1 = WS_KV + (4u << 20);
constexpr size_t WS_WT = WS_X1 + (2u << 20);
constexpr size_t WS_OF = WS_WT + (2u << 20);
constexpr size_t WS_Z2 = WS_OF + (2u << 20);

DI unsigned pk(float a, float b) { f32x2 v = {a, b}; return __builtin_bit_cast(unsigned, __builtin_convertvector(v, bf16x2_t)); }
DI u32x4 pk8(const f32x16& x, int s) { u32x4 r; r[0] = pk(x[8 * s], x[8 * s + 1]); r[1] = pk(x[8 * s + 2], x[8 * s + 3]); r[2] = pk(x[8 * s + 4], x[8 * s + 5]); r[3] = pk(x[8 * s + 6], x[8 * s + 7]); return r; }
DI bf16x8 asb(u32x4 v) { return __builtin_bit_cast(bf16x8, v); }
DI bf16x8 ldsr(const LDS_AS char* p) { return *(const LDS_AS bf16x8*)p; }
DI s16x4 ldtr(const LDS_AS char* p) { return __builtin_amdgcn_ds_read_tr16_b64_v4i16((LDS_AS s16x4*)p); }
DI bf16x8 ldtr2(const LDS_AS char* p, int off2) { s16x4 lo = ldtr(p), hi = ldtr(p + off2); return __builtin_shufflevector(lo, hi, 0, 1, 2, 3, 4, 5, 6, 7); }
DI void dma16(const char* g, LDS_AS char* l) { __builtin_amdgcn_global_load_lds((const __attribute__((address_space(1))) unsigned*)g, (LDS_AS unsigned*)l, 16, 0, 0); }
#define WAITV(n) asm volatile("s_waitcnt vmcnt(" #n ")" ::: "memory")
#define BARRIER() asm volatile("s_waitcnt lgkmcnt(0)\n\ts_barrier" ::: "memory")
DI const f32x16 zero16() { f32x16 z; for (int i = 0; i < 16; ++i) z[i] = 0.f; return z; }

template <bool TRANS>
DI void k1_loop(const LDS_AS char* lds, int lane, int wm, int wn, f32x16 (&acc)[2][2]) {
  const int r = lane & 31, h = lane >> 5;
  const int sw = (r >> 1) & 7;
  const LDS_AS char* xa = lds + (wm * 64 + r) * 128;
  int xo[4];
#pragma unroll
  for (int s = 0; s < 4; ++s) xo[s] = ((2 * s + h) ^ sw) * 16;
  const int q = (lane & 15) >> 2;
  const LDS_AS char* wb0 = lds + 16384 + (8 * h + q) * 256 + (((2 * wn) ^ q) * 64) + ((lane >> 4) & 1) * 32 + (lane & 3) * 8;
  const LDS_AS char* wb1 = lds + 16384 + (8 * h + q) * 256 + (((2 * wn + 1) ^ q) * 64) + ((lane >> 4) & 1) * 32 + (lane & 3) * 8;
  bf16x8 a0, a1, b0, b1, na0, na1, nb0, nb1;
  BARRIER();
  a0 = ldsr(xa + xo[0]); a1 = ldsr(xa + xo[0] + 4096); b0 = ldtr2(wb0, 1024); b1 = ldtr2(wb1, 1024);
  int so = 0;
  for (int c = 0; c < 7; ++c) {
#pragma unroll
    for (int s = 0; s < 4; ++s) {
      if (s < 3) {
        na0 = ldsr(xa + so + xo[s + 1]); na1 = ldsr(xa + so + xo[s + 1] + 4096);
        nb0 = ldtr2(wb0 + so + (s + 1) * 4096, 1024); nb1 = ldtr2(wb1 + so + (s + 1) * 4096, 1024);
      } else if (c < 6) {
        BARRIER();
        so = (so == 4 * 32768) ? 0 : so + 32768;
        na0 = ldsr(xa + so + xo[0]); na1 = ldsr(xa + so + xo[0] + 4096);
        nb0 = ldtr2(wb0 + so, 1024); nb1 = ldtr2(wb1 + so, 1024);
      }
      if (TRANS) {
        acc[0][0] = MFMA(b0, a0, acc[0][0]); acc[0][1] = MFMA(b1, a0, acc[0][1]);
        acc[1][0] = MFMA(b0, a1, acc[1][0]); acc[1][1] = MFMA(b1, a1, acc[1][1]);
      } else {
        acc[0][0] = MFMA(a0, b0, acc[0][0]); acc[0][1] = MFMA(a0, b1, acc[0][1]);
        acc[1][0] = MFMA(a1, b0, acc[1][0]); acc[1][1] = MFMA(a1, b1, acc[1][1]);
      }
      a0 = na0; a1 = na1; b0 = nb0; b1 = nb1;
    }
  }
}

__global__ __launch_bounds__(512, 2) void k1_proj(const u16* __restrict__ ax, const u16* __restrict__ wr, const u16* __restrict__ wi,
                                                  const u16* __restrict__ bx, const u16* __restrict__ wqkv, char* __restrict__ ws) {
  __shared__ __attribute__((aligned(1024))) char smem[5 * 32768];
  LDS_AS char* lds = (LDS_AS char*)smem;
  const int bid = blockIdx.x, xcd = bid & 7, jj = bid >> 3;
  const int mt = 8 * (xcd & 1) + (jj & 7), nt = 4 * (xcd >> 1) + (jj >> 3), type = nt >> 2;
  const int lane = threadIdx.x & 63, wave = __builtin_amdgcn_readfirstlane(threadIdx.x >> 6);
  if (wave >= 4) {
    const int lw = wave - 4;
    unsigned wv[2][8]; int wc[2], wf1[2];
    { const int a = bid >> 4, o = (bid >> 2) & 3, cg = bid & 3;
#pragma unroll
      for (int k = 0; k < 2; ++k) {
        int u = lw * 64 + lane + 256 * k; u = u > 455 ? 455 : u;
        const int f1l = (u * 1150) >> 16, cl = u - 57 * f1l;
        int c = 57 * cg + cl; c = c > 224 ? 224 : c;
        wc[k] = c; wf1[k] = 8 * o + f1l;
        const int off = ((128 * a + 8 * o + f1l) * DF + c) * 2;
#pragma unroll
        for (int i = 0; i < 4; ++i) {
          asm volatile("global_load_ushort %0, %1, %2" : "=v"(wv[k][2 * i]) : "v"(off + i * 32 * DF * 2), "s"(wr));
          asm volatile("global_load_ushort %0, %1, %2" : "=v"(wv[k][2 * i + 1]) : "v"(off + i * 32 * DF * 2), "s"(wi));
        }
      } }
    const char* xb = (const char*)(type == 3 ? ax : bx);
    const char* wbase = type == 3 ? (const char*)&g_F + (nt - 12) * 256 : (const char*)wqkv + nt * 224;
    const int ldb = type == 3 ? 896 : 2688, blk = type == 3 ? 64 : 56, last = type == 3 ? 48 : 40;
    unsigned vx[4], vw[4];
#pragma unroll
    for (int i = 0; i < 4; ++i) {
      const int p = lw * 4 + i;
      const int row = 8 * p + (lane >> 3), kg = (lane & 7) ^ ((row >> 1) & 7);
      const int tok = type == 3 ? 64 * (row & 31) + 4 * mt + (row >> 5) : 128 * mt + row;
      vx[i] = tok * 896 + kg * 16;
      const int wrow = 4 * p + (lane >> 4), b = ((lane >> 2) & 3) ^ (wrow & 3), pi = lane & 3;
      vw[i] = wrow * ldb + b * blk + (pi == 3 ? last : pi * 16);
    }
    auto issue = [&](int c, int slot) {
      const char* xs = xb + c * 128; const char* wsrc = wbase + c * 64 * ldb;
      LDS_AS char* l = lds + slot * 32768 + lw * 4096;
#pragma unroll
      for (int i = 0; i < 4; ++i) dma16(xs + vx[i], l + i * 1024);
#pragma unroll
      for (int i = 0; i < 4; ++i) dma16(wsrc + vw[i], l + 16384 + i * 1024);
    };
#pragma unroll
    for (int c = 0; c < 5; ++c) issue(c, c);
    asm volatile("s_waitcnt vmcnt(32)\n\ts_barrier"
                 : "+v"(wv[0][0]), "+v"(wv[0][1]), "+v"(wv[0][2]), "+v"(wv[0][3]), "+v"(wv[0][4]), "+v"(wv[0][5]), "+v"(wv[0][6]), "+v"(wv[0][7]),
                   "+v"(wv[1][0]), "+v"(wv[1][1]), "+v"(wv[1][2]), "+v"(wv[1][3]), "+v"(wv[1][4]), "+v"(wv[1][5]), "+v"(wv[1][6]), "+v"(wv[1][7]) :: "memory");
    { const int a = bid >> 4;
#pragma unroll
      for (int k = 0; k < 2; ++k) {
        u32x4 o;
        const bool half = (wc[k] == 0) || (wc[k] == 224);
#pragma unroll
        for (int i = 0; i < 4; ++i) {
          unsigned re = wv[k][2 * i] << 16, im = wv[k][2 * i + 1] << 16;
          if (half) { re = __float_as_uint(__uint_as_float(re) * 0.5f); im = __float_as_uint(__uint_as_float(im) * 0.5f); }
          o[i] = (re >> 16) | (im & 0xffff0000u);
        }
        *(u32x4*)(ws + WS_WT + wc[k] * 8192 + ((a >> 1) * 64 + wf1[k] + 32 * (a & 1)) * 16) = o;
      } }
    asm volatile("s_waitcnt vmcnt(26)\n\ts_barrier" ::: "memory");
    issue(5, 0);
    asm volatile("s_waitcnt vmcnt(26)\n\ts_barrier" ::: "memory");
    issue(6, 1);
    asm volatile("s_waitcnt vmcnt(26)\n\ts_barrier" ::: "memory");
    asm volatile("s_waitcnt vmcnt(18)\n\ts_barrier" ::: "memory");
    asm volatile("s_waitcnt vmcnt(8)\n\ts_barrier" ::: "memory");
    asm volatile("s_waitcnt vmcnt(0)\n\ts_barrier" ::: "memory");
    return;
  }
  const int wm = wave & 1, wn = wave >> 1, r = lane & 31, h = lane >> 5;
  f32x16 acc[2][2];
#pragma unroll
  for (int i = 0; i < 2; ++i)
#pragma unroll
    for (int j = 0; j < 2; ++j) acc[i][j] = zero16();
  if (type < 2) k1_loop<true>(lds, lane, wm, wn, acc); else k1_loop<false>(lds, lane, wm, wn, acc);
#pragma unroll
  for (int mi = 0; mi < 2; ++mi)
#pragma unroll
    for (int ni = 0; ni < 2; ++ni) {
      f32x16 v = acc[mi][ni];
      const int blk = 2 * wn + ni, tt = 4 * mt + 2 * wm + mi, head = 4 * (nt & 3) + blk;
      u32x4 p0, p1; char* dst;
      if (type == 0) {
        const float qs = 0.18898223650461362f * 1.4426950408889634f;
#pragma unroll
        for (int i = 0; i < 16; ++i) v[i] *= qs;
        p0 = pk8(v, 0); p1 = pk8(v, 1);
        if (h == 0) { p1[2] = 0; p1[3] = 0; }
        dst = ws + WS_QF + (head * 64 + tt) * 2048;
      } else if (type == 1) {
        p0 = pk8(v, 0); p1 = pk8(v, 1);
        if (h == 0) { p1[2] = 0x3f80u; p1[3] = 0; }
        dst = ws + WS_KV + (head * 64 + tt) * 4096;
      } else if (type == 2) {
        p0 = pk8(v, 0); p1 = pk8(v, 1);
        if ((r >> 2) == 6) { const unsigned c = r == 24 ? 0x3f803f80u : 0u; p0 = (u32x4){c, c, c, c}; p1 = p0; }
        dst = ws + WS_KV + (head * 64 + tt) * 4096 + 2048;
      } else {
        p0 = pk8(v, 0); p1 = pk8(v, 1);
        const int j = 4 * (nt - 12) + blk;
        if (j >= 14) continue;
        dst = ws + WS_X1 + (j * 64 + tt) * 2048;
      }
      *(u32x4*)(dst + lane * 16) = p0; *(u32x4*)(dst + 1024 + lane * 16) = p1;
    }
}

DI float swapmax(float m) { auto rr = __builtin_amdgcn_permlane32_swap(__float_as_uint(m), __float_as_uint(m), false, false); return fmaxf(__uint_as_float(rr[0]), __uint_as_float(rr[1])); }
DI float max16(const f32x16& s) {
  float a = fmaxf(fmaxf(s[0], s[1]), s[2]), b = fmaxf(fmaxf(s[3], s[4]), s[5]);
  a = fmaxf(fmaxf(a, s[6]), s[7]); b = fmaxf(fmaxf(b, s[8]), s[9]); a = fmaxf(fmaxf(a, s[10]), s[11]); b = fmaxf(fmaxf(b, s[12]), s[13]);
  a = fmaxf(fmaxf(a, s[14]), s[15]); return fmaxf(a, b); }
#define EX2(x) __builtin_amdgcn_exp2f(x)

DI void a2_column(int c, int lane, const char* __restrict__ ws_in, char* __restrict__ ws, LDS_AS char* tl) {
  const int r = lane & 31, h = lane >> 5;
  const bool real_col = (c == 0) || (c == 224);
  const int cp = c == 224 ? 1 : 2 * c;
  const char* xsrc = ws_in + WS_X1 + ((cp >> 5) * 64 + r) * 2048 + ((cp & 31) + 32 * h) * 16;
  const char* tb = (const char*)&g_A2 + lane * 16;
  const char* twb = (const char*)&g_TW + lane * 16;
#define T32F(mat, t) (*(const bf16x8*)(tb + ((mat) * 2 + (t)) * 1024))
#define T64(mat, m, k) (*(const bf16x8*)(tb + 6144 + (((mat) * 2 + (m)) * 4 + (k)) * 1024))
#define T32I(mat, t) (*(const bf16x8*)(tb + 6144 + 24576 + ((mat) * 2 + (t)) * 1024))
#define TW(m, g, p) (*(const f32x4*)(twb + (((m) * 4 + (g)) * 2 + (p)) * 1024))
  const char* tb2 = tb; asm volatile("" : "+v"(tb2));
  const char* twb2 = twb; asm volatile("" : "+v"(twb2));
#define T64V(mat, m, k) (*(const bf16x8*)(tb2 + 6144 + (((mat) * 2 + (m)) * 4 + (k)) * 1024))
#define TWV(m, g, p) (*(const f32x4*)(twb2 + (((m) * 4 + (g)) * 2 + (p)) * 1024))
  bf16x8 ur[2][2], ui[2][2], t32[3][2], t64[3][2][4], t64b[3][4]; f32x4 tw[2][4][2]; u32x4 wv[2][4];
#pragma unroll
  for (int m = 0; m < 2; ++m)
#pragma unroll
    for (int t = 0; t < 2; ++t) {
      ur[m][t] = *(const bf16x8*)(xsrc + m * 65536 + t * 1024);
      ui[m][t] = *(const bf16x8*)(xsrc + m * 65536 + t * 1024 + (real_col ? 0 : 16));
    }
#pragma unroll
  for (int i = 0; i < 6; ++i) t32[i >> 1][i & 1] = T32F(i >> 1, i & 1);
#pragma unroll
  for (int i = 0; i < 16; ++i) tw[i >> 3][(i >> 1) & 3][i & 1] = TW(i >> 3, (i >> 1) & 3, i & 1);
  SBAR();
  if (real_col) {
#pragma unroll
    for (int m = 0; m < 2; ++m)
#pragma unroll
      for (int t = 0; t < 2; ++t) ui[m][t] = (bf16x8){0, 0, 0, 0, 0, 0, 0, 0};
  }
  f32x16 re[2], im[2];
#pragma unroll
  for (int m = 0; m < 2; ++m) {
    re[m] = MFMA(ur[m][0], t32[0][0], zero16()); im[m] = MFMA(ui[m][0], t32[0][0], zero16());
    re[m] = MFMA(ur[m][1], t32[0][1], re[m]);    im[m] = MFMA(ui[m][1], t32[0][1], im[m]);
    re[m] = MFMA(ui[m][0], t32[1][0], re[m]);    im[m] = MFMA(ur[m][0], t32[2][0], im[m]);
    re[m] = MFMA(ui[m][1], t32[1][1], re[m]);    im[m] = MFMA(ur[m][1], t32[2][1], im[m]);
  }
  SBAR();
#pragma unroll
  for (int i = 0; i < 12; ++i) t64[i >> 2][0][i & 3] = T64(i >> 2, 0, i & 3);
  SBAR();
  u32x4 br[4], bi[4];
#pragma unroll
  for (int m = 0; m < 2; ++m) {
#pragma unroll
    for (int g = 0; g < 4; ++g) { const f32x4 cs = tw[m][g][0], sn = tw[m][g][1];
#pragma unroll
      for (int i = 0; i < 4; ++i) { const float a = re[m][4 * g + i], b = im[m][4 * g + i];
        re[m][4 * g + i] = a * cs[i] + b * sn[i]; im[m][4 * g + i] = b * cs[i] - a * sn[i]; } }
    br[2 * m] = pk8(re[m], 0); br[2 * m + 1] = pk8(re[m], 1); bi[2 * m] = pk8(im[m], 0); bi[2 * m + 1] = pk8(im[m], 1);
  }
  SBAR();
#pragma unroll
  for (int i = 0; i < 12; ++i) t64[i >> 2][1][i & 3] = T64(i >> 2, 1, i & 3);
  { const char* wt = ws_in + WS_WT + c * 8192 + lane * 16;
#pragma unroll
    for (int i = 0; i < 8; ++i) wv[i >> 2][i & 3] = *(const u32x4*)(wt + i * 1024); }
  SBAR();
#pragma unroll
  for (int m = 0; m < 2; ++m) {
    re[m] = zero16(); im[m] = zero16();
#pragma unroll
    for (int k = 0; k < 4; ++k) {
      re[m] = MFMA(t64[0][m][k], asb(br[k]), re[m]); im[m] = MFMA(t64[0][m][k], asb(bi[k]), im[m]);
      re[m] = MFMA(t64[1][m][k], asb(bi[k]), re[m]); im[m] = MFMA(t64[2][m][k], asb(br[k]), im[m]);
    }
  }
  SBAR();
#pragma unroll
  for (int i = 0; i < 12; ++i) t64b[i >> 2][i & 3] = T64V(i >> 2, 0, i & 3);
  SBAR();
#pragma unroll
  for (int m = 0; m < 2; ++m) {
#pragma unroll
    for (int g = 0; g < 4; ++g) { const u32x4 w = wv[m][g];
#pragma unroll
      for (int i = 0; i < 4; ++i) { const float a = re[m][4 * g + i], b = im[m][4 * g + i];
        const float wre = __uint_as_float(w[i] << 16), wim = __uint_as_float(w[i] & 0xffff0000u);
        re[m][4 * g + i] = a * wre - b * wim; im[m][4 * g + i] = a * wim + b * wre; } }
    br[2 * m] = pk8(re[m], 0); br[2 * m + 1] = pk8(re[m], 1); bi[2 * m] = pk8(im[m], 0); bi[2 * m + 1] = pk8(im[m], 1);
  }
  SBAR();
#pragma unroll
  for (int i = 0; i < 8; ++i) tw[0][i >> 1][i & 1] = TWV(0, i >> 1, i & 1);
  SBAR();
#pragma unroll
  for (int m = 0; m < 2; ++m) {
    re[m] = zero16(); im[m] = zero16();
#pragma unroll
    for (int k = 0; k < 4; ++k) {
      const bf16x8 C = m ? t64[0][1][k] : t64b[0][k], S = m ? t64[1][1][k] : t64b[1][k], N = m ? t64[2][1][k] : t64b[2][k];
      re[m] = MFMA(C, asb(br[k]), re[m]); im[m] = MFMA(C, asb(bi[k]), im[m]);
      re[m] = MFMA(N, asb(bi[k]), re[m]); im[m] = MFMA(S, asb(br[k]), im[m]);
    }
    SBAR();
    if (m == 0) {
#pragma unroll
      for (int i = 0; i < 8; ++i) tw[1][i >> 1][i & 1] = TWV(1, i >> 1, i & 1);
      SBAR();
    }
  }
#pragma unroll
  for (int i = 0; i < 6; ++i) t32[i >> 1][i & 1] = T32I(i >> 1, i & 1);
  SBAR();
#pragma unroll
  for (int m = 0; m < 2; ++m)
#pragma unroll
    for (int g = 0; g < 4; ++g) { const f32x4 cs = tw[m][g][0], sn = tw[m][g][1];
      float xr[4], xi[4];
#pragma unroll
      for (int i = 0; i < 4; ++i) { const float a = re[m][4 * g + i], b = im[m][4 * g + i];
        xr[i] = a * cs[i] - b * sn[i]; xi[i] = b * cs[i] + a * sn[i]; }
      LDS_AS char* p = tl + r * 192 + (32 * m + 8 * g + 4 * h) * 2;
      *(LDS_AS u32x2*)p = (u32x2){pk(xr[0], xr[1]), pk(xr[2], xr[3])};
      *(LDS_AS u32x2*)(p + 6144) = (u32x2){pk(xi[0], xi[1]), pk(xi[2], xi[3])};
    }
  asm volatile("s_waitcnt lgkmcnt(0)" ::: "memory");
  const LDS_AS char* tp = tl + (8 * h + ((lane & 15) >> 2)) * 192 + ((lane >> 4) & 1) * 32 + (lane & 3) * 8;
#pragma unroll
  for (int m = 0; m < 2; ++m) {
    re[m] = zero16(); im[m] = zero16();
#pragma unroll
    for (int t = 0; t < 2; ++t) {
      const bf16x8 ar = ldtr2(tp + t * 16 * 192 + m * 64, 4 * 192), ai = ldtr2(tp + 6144 + t * 16 * 192 + m * 64, 4 * 192);
      re[m] = MFMA(ar, t32[0][t], re[m]); im[m] = MFMA(ai, t32[0][t], im[m]);
      re[m] = MFMA(ai, t32[2][t], re[m]); im[m] = MFMA(ar, t32[1][t], im[m]);
    }
  }
  if (!real_col) {
    char* z = ws + WS_Z2 + c * 8192 + lane * 16;
#pragma unroll
    for (int m = 0; m < 2; ++m)
#pragma unroll
      for (int g = 0; g < 4; ++g)
        *(u32x4*)(z + (m * 4 + g) * 1024) = (u32x4){pk(re[m][4 * g], im[m][4 * g]), pk(re[m][4 * g + 1], im[m][4 * g + 1]), pk(re[m][4 * g + 2], im[m][4 * g + 2]), pk(re[m][4 * g + 3], im[m][4 * g + 3])};
  } else {
    char* z = ws + WS_Z2 + lane * 16 + (c ? 2 : 0);
#pragma unroll
    for (int m = 0; m < 2; ++m)
#pragma unroll
      for (int g = 0; g < 16; ++g) *(u16*)(z + (m * 4 + (g >> 2)) * 1024 + (g & 3) * 4) = (u16)pk(re[m][g], 0.f);
  }
}

constexpr int K2_EX = 0, K2_MX = 65536, K2_CNT = K2_MX + 2048, K2_TL = K2_CNT + 64, K2_LDS = K2_TL + 12288;

#define A_MF(d, a, b, c) "v_mfma_f32_32x32x16_bf16 v[" #d ":" #d "+15], v[" #a ":" #a "+3], v[" #b ":" #b "+3], " c "\n\t"
#define A_ACC(d) "v[" #d ":" #d "+15]"
#define A_EXP(s, p) "v_exp_f32 v168, v[" #s "]\n\tv_exp_f32 v169, v[" #s "+1]\n\tv_exp_f32 v170, v[" #s "+2]\n\tv_exp_f32 v171, v[" #s "+3]\n\t" \
                    "v_cvt_pk_bf16_f32 v[" #p "], v168, v169\n\tv_cvt_pk_bf16_f32 v[" #p "+1], v170, v171\n\t"
#define A_PERIOD(SC, SN, KN, QN, OC, V0C, OP, V1P, W3) \
  A_MF(SN, KN, QN, "0")                 A_EXP(SC, 160) \
  A_MF(OP, V1P, 164, A_ACC(OP))         A_EXP(SC + 4, 162) \
  A_MF(SN, KN + 4, QN + 4, A_ACC(SN))   A_EXP(SC + 8, 164) \
  W3 A_MF(OC, V0C, 160, A_ACC(OC))      A_EXP(SC + 12, 166)
#define A_ADDR(idx) "s_add_i32 s40, " idx ", %[rot]\n\ts_and_b32 s40, s40, 63\n\ts_lshl_b32 s40, s40, 12\n\ts_add_u32 s42, %[kvlo], s40\n\ts_addc_u32 s43, %[kvhi], 0\n\t"
#define A_TICKET "s_mov_b64 exec, 1\n\tds_add_rtn_u32 v175, v173, v174\n\ts_mov_b64 exec, -1\n\t"
#define A_HALF(L, KC, VC, KO, VO, IO, IN)   \
  A_TICKET \
  A_PERIOD(64, 80, KC, 104, 0, VC, 48, VO + 4, "s_waitcnt vmcnt(2)\n\t") \
  "s_cmp_ge_i32 " IO ", 64\n\ts_cbranch_scc1 " L "a%=\n\t" A_ADDR(IO) \
  "global_load_dwordx4 v[" #VO ":" #VO "+3], v172, s[42:43] offset:2048\n\tglobal_load_dwordx4 v[" #VO "+4:" #VO "+7], v172, s[42:43] offset:3072\n" \
  L "a%=:\n\t" \
  A_PERIOD(80, 64, KC, 112, 16, VC, 0, VC + 4, "") \
  A_PERIOD(64, 80, KC, 120, 32, VC, 16, VC + 4, "") \
  "s_waitcnt lgkmcnt(0)\n\tv_readfirstlane_b32 " IN ", v175\n\ts_nop 0\n\ts_cmp_ge_i32 " IN ", 64\n\ts_cbranch_scc1 " L "b%=\n\t" A_ADDR(IN) \
  "global_load_dwordx4 v[" #KC ":" #KC "+3], v172, s[42:43]\n\tglobal_load_dwordx4 v[" #KC "+4:" #KC "+7], v172, s[42:43] offset:1024\n\t" \
  "s_waitcnt vmcnt(4)\n\ts_branch " L "c%=\n" \
  L "b%=:\n\ts_waitcnt vmcnt(0)\n" \
  L "c%=:\n\t" \
  A_PERIOD(80, 64, KO, 96, 48, VC, 32, VC + 4, "")

__global__ __launch_bounds__(320, 2) void k2_attn(const char* __restrict__ wsi, char* __restrict__ ws) {
  __shared__ __attribute__((aligned(1024))) char smem[K2_LDS];
  LDS_AS char* lds = (LDS_AS char*)smem;
  const int bid = blockIdx.x, xcd = bid & 7, jj = bid >> 3;
  const int head = 2 * xcd + (jj & 1), qb = jj >> 1;
  const int lane = threadIdx.x & 63, wave = __builtin_amdgcn_readfirstlane(threadIdx.x >> 6);
  const int h = lane >> 5;
  if (wave == 4) {
    __builtin_amdgcn_s_setprio(2);
    if (lane == 0) *(LDS_AS int*)(lds + K2_CNT) = 8;
    BARRIER();
    const int c = (jj >> 2) * 32 + xcd * 4 + (jj & 3);
    if (c < DF) a2_column(c, lane, wsi, ws, lds + K2_TL);
    return;
  }
  const char* qsrc = wsi + WS_QF + (head * 64 + 4 * qb) * 2048 + lane * 16;
  const char* kvb = wsi + WS_KV + head * 64 * 4096 + lane * 16;
  const int rot = 4 * qb;
  bf16x8 KA0, KA1, VA0, VA1, KB0, KB1;
#define LDK(K0, K1, i) do { const char* p_ = kvb + ((((i) + rot) & 63) << 12); K0 = *(const bf16x8*)p_; K1 = *(const bf16x8*)(p_ + 1024); } while (0)
#define LDV(V0, V1, i) do { const char* p_ = kvb + ((((i) + rot) & 63) << 12); V0 = *(const bf16x8*)(p_ + 2048); V1 = *(const bf16x8*)(p_ + 3072); } while (0)
  LDK(KA0, KA1, wave);
  bf16x8 Q0[4], Q1[4];
#pragma unroll
  for (int t = 0; t < 4; ++t) { Q0[t] = *(const bf16x8*)(qsrc + t * 2048); Q1[t] = *(const bf16x8*)(qsrc + t * 2048 + 1024); }
  LDV(VA0, VA1, wave); LDK(KB0, KB1, wave + 4);
  BARRIER();
  f32x16 O[4]; float mref[4];
#pragma unroll
  for (int t = 0; t < 4; ++t) {
    f32x16 s = MFMA(KA0, Q0[t], zero16()); s = MFMA(KA1, Q1[t], s);
    const float m = swapmax(max16(s));
    const unsigned nb = pk(-m, 0.f);
    mref[t] = -__uint_as_float(nb << 16);
    u32x4 q = __builtin_bit_cast(u32x4, Q1[t]); q[2] = h == 0 ? nb : q[2]; Q1[t] = asb(q);
    O[t] = zero16();
  }
  {
    f32x16 SA = MFMA(KA0, Q0[0], zero16()); SA = MFMA(KA1, Q1[0], SA);
    const uint64_t kvs = (uint64_t)(wsi + WS_KV + head * 64 * 4096);
    const unsigned kvlo = (unsigned)kvs, kvhi = (unsigned)(kvs >> 32);
    int ib = wave + 4, ic;
    u32x4 VB1 = {0, 0, 0, 0}, P1 = {0, 0, 0, 0};
    asm volatile(
      "s_nop 7\n"
      "Ltop%=:\n\t"
      A_HALF("La", 128, 136, 144, 152, "%[ib]", "%[ic]")
      "s_cmp_ge_i32 %[ib], 64\n\ts_cbranch_scc1 LendA%=\n\t"
      A_HALF("Lb", 144, 152, 128, 136, "%[ic]", "%[ib]")
      "s_cmp_lt_i32 %[ic], 64\n\ts_cbranch_scc1 Ltop%=\n\t"
      A_MF(48, 156, 164, A_ACC(48))
      "s_branch Ldone%=\n"
      "LendA%=:\n\t"
      A_MF(48, 140, 164, A_ACC(48))
      "Ldone%=:\n\t"
      "s_nop 11"
      : "+{v[0:15]}"(O[0]), "+{v[16:31]}"(O[1]), "+{v[32:47]}"(O[2]), "+{v[48:63]}"(O[3]), "+{v[64:79]}"(SA),
        "+{v[128:131]}"(KA0), "+{v[132:135]}"(KA1), "+{v[136:139]}"(VA0), "+{v[140:143]}"(VA1), "+{v[144:147]}"(KB0), "+{v[148:151]}"(KB1),
        "+{v[156:159]}"(VB1), "+{v[164:167]}"(P1), [ib] "+s"(ib), [ic] "=&s"(ic)
      : "{v[96:99]}"(Q0[0]), "{v[100:103]}"(Q1[0]), "{v[104:107]}"(Q0[1]), "{v[108:111]}"(Q1[1]), "{v[112:115]}"(Q0[2]), "{v[116:119]}"(Q1[2]), "{v[120:123]}"(Q0[3]), "{v[124:127]}"(Q1[3]),
        "{v172}"(lane * 16), "{v173}"((unsigned)(uintptr_t)(lds + K2_CNT)), "{v174}"(1),
        [rot] "s"(rot), [kvlo] "s"(kvlo), [kvhi] "s"(kvhi)
      : "memory", "scc", "s40", "s42", "s43", "v80", "v81", "v82", "v83", "v84", "v85", "v86", "v87", "v88", "v89", "v90", "v91", "v92", "v93", "v94", "v95",
        "v152", "v153", "v154", "v155", "v160", "v161", "v162", "v163", "v168", "v169", "v170", "v171", "v175");
  }
#pragma unroll
  for (int t = 0; t < 4; ++t) {
    if (t != wave) {
      LDS_AS char* e = lds + K2_EX + (wave * 4 + t) * 4096 + lane * 16;
#pragma unroll
      for (int g = 0; g < 4; ++g) *(LDS_AS f32x4*)(e + g * 1024) = (f32x4){O[t][4 * g], O[t][4 * g + 1], O[t][4 * g + 2], O[t][4 * g + 3]};
      *(LDS_AS float*)(lds + K2_MX + (wave * 4 + t) * 128 + (lane & 31) * 4) = mref[t];
    }
  }
  f32x16 acc; float mo;
  if (wave == 0) { acc = O[0]; mo = mref[0]; } else if (wave == 1) { acc = O[1]; mo = mref[1]; } else if (wave == 2) { acc = O[2]; mo = mref[2]; } else { acc = O[3]; mo = mref[3]; }
  BARRIER();
  {
    float mw[3]; float M = mo;
#pragma unroll
    for (int k = 0; k < 3; ++k) { const int w = (wave + 1 + k) & 3; mw[k] = *(const LDS_AS float*)(lds + K2_MX + (w * 4 + wave) * 128 + (lane & 31) * 4); M = fmaxf(M, mw[k]); }
    bool ok = (acc[12] >= 0x1p-40f && acc[12] <= 0x1p40f);
    const float fo = EX2(mo - M);
#pragma unroll
    for (int i = 0; i < 16; ++i) acc[i] *= fo;
#pragma unroll
    for (int k = 0; k < 3; ++k) { const int w = (wave + 1 + k) & 3; const float f = EX2(mw[k] - M);
      const LDS_AS char* e = lds + K2_EX + (w * 4 + wave) * 4096 + lane * 16;
#pragma unroll
      for (int g = 0; g < 4; ++g) { const f32x4 v = *(const LDS_AS f32x4*)(e + g * 1024);
        if (g == 3) ok = ok && (v[0] >= 0x1p-40f && v[0] <= 0x1p40f);
#pragma unroll
        for (int i = 0; i < 4; ++i) acc[4 * g + i] = fmaf(v[i], f, acc[4 * g + i]); } }
    ok = ok || h == 1;
    float am = 0.f;
#pragma unroll
    for (int i = 0; i < 16; i += 2) am = fmaxf(fmaxf(am, fabsf(acc[i])), fabsf(acc[i + 1]));
    float sm = 0.f;
#pragma unroll
    for (int i = 0; i < 16; ++i) sm += acc[i] * 0.f;
    ok = ok && (sm == 0.f);
    (void)am;
    if (__builtin_expect(!__all(ok), 0)) {
      bf16x8 q0 = *(const bf16x8*)(qsrc + wave * 2048), q1 = *(const bf16x8*)(qsrc + wave * 2048 + 1024);
      float m = -INFINITY; acc = zero16();
      for (int kt = 0; kt < 64; ++kt) {
        const char* p = kvb + (kt << 12);
        f32x16 s = MFMA(*(const bf16x8*)p, q0, zero16()); s = MFMA(*(const bf16x8*)(p + 1024), q1, s);
        const float mn = fmaxf(m, swapmax(max16(s)));
        const float al = EX2(m - mn); m = mn;
#pragma unroll
        for (int i = 0; i < 16; ++i) { acc[i] *= al; s[i] = EX2(s[i] - mn); }
        acc = MFMA(*(const bf16x8*)(p + 2048), asb(pk8(s, 0)), acc); acc = MFMA(*(const bf16x8*)(p + 3072), asb(pk8(s, 1)), acc);
      }
    }
    auto rr = __builtin_amdgcn_permlane32_swap(__float_as_uint(acc[12]), __float_as_uint(acc[12]), false, false);
    const float inv = 1.0f / __uint_as_float(rr[0]);
#pragma unroll
    for (int i = 0; i < 16; ++i) acc[i] *= inv;
    u32x4 p0 = pk8(acc, 0), p1 = pk8(acc, 1);
    if (h == 0) { p1[2] = 0; p1[3] = 0; }
    char* dst = ws + WS_OF + ((4 * qb + wave) * 16 + head) * 2048 + lane * 16;
    *(u32x4*)dst = p0; *(u32x4*)(dst + 1024) = p1;
  }
}

__global__ __launch_bounds__(512, 2) void k3_out(const char* __restrict__ ws, const u16* __restrict__ wo, u16* __restrict__ out) {
  __shared__ __attribute__((aligned(1024))) char smem[57344];
  LDS_AS char* lds = (LDS_AS char*)smem;
  const int bid = blockIdx.x, xcd = bid & 7, jj = bid >> 3;
  const int part = xcd & 1, mt = 4 * (xcd >> 1) + (jj & 3), nt = jj >> 2;
  const int lane = threadIdx.x & 63, wave = __builtin_amdgcn_readfirstlane(threadIdx.x >> 6);
  if (wave >= 4) {
    const int lw = wave - 4, rr = lane >> 3, sl = lane & 7;
    const char* src; int pstride;
    if (part) { src = (const char*)wo + nt * 128 + rr * 896 + (((sl >> 2) ^ ((rr >> 1) & 1)) * 64) + (sl & 3) * 16 + lw * 8 * 896; pstride = 32 * 896; }
    else { src = nullptr; pstride = 0; }
#pragma unroll
    for (int i = 0; i < 14; ++i) {
      const int p = lw + 4 * i;
      const char* g;
      if (part) g = src + i * pstride;
      else { const int seg = p >> 3, row = 8 * (p & 7) + rr; g = (const char*)&g_F + (64 * nt + row) * 896 + seg * 128 + ((sl ^ ((row >> 1) & 7)) * 16); }
      dma16(g, lds + p * 1024);
    }
    asm volatile("s_waitcnt vmcnt(10)\n\ts_barrier" ::: "memory");
    asm volatile("s_waitcnt vmcnt(6)\n\ts_barrier" ::: "memory");
    asm volatile("s_waitcnt vmcnt(2)\n\ts_barrier" ::: "memory");
    asm volatile("s_waitcnt vmcnt(0)\n\ts_barrier" ::: "memory");
    return;
  }
  const int r = lane & 31, h = lane >> 5, tt = 4 * mt + wave;
  f32x16 acc0 = zero16(), acc1 = zero16();
  int tok, colb;
  if (part) {
    bf16x8 tk[32];
    const char* src = ws + WS_OF + tt * 32768 + lane * 16;
#pragma unroll
    for (int s = 0; s < 32; ++s) tk[s] = *(const bf16x8*)(src + s * 1024);
    const int q = (lane & 15) >> 2;
    const LDS_AS char* w0 = lds + (4 * h + q) * 128 + ((q >> 1) * 64) + ((lane >> 4) & 1) * 32 + (lane & 3) * 8;
    const LDS_AS char* w1 = lds + (4 * h + q) * 128 + (((q >> 1) ^ 1) * 64) + ((lane >> 4) & 1) * 32 + (lane & 3) * 8;
#pragma unroll
    for (int s = 0; s < 32; ++s) {
      if ((s & 7) == 0) asm volatile("s_barrier" ::: "memory");
      const int o = (s >> 1) * 3584 + (s & 1) * 2048, o2 = (s & 1) ? 512 : 1024;
      acc0 = MFMA(ldtr2(w0 + o, o2), tk[s], acc0); acc1 = MFMA(ldtr2(w1 + o, o2), tk[s], acc1);
    }
    tok = 32 * tt + r; colb = 448 + 64 * nt;
  } else {
    u32x4 tk[28];
    const char* src = ws + WS_Z2 + h * 4 * 8192 + (32 * tt + r) * 4;
#pragma unroll
    for (int s = 0; s < 28; ++s)
#pragma unroll
      for (int i = 0; i < 4; ++i) tk[s][i] = *(const unsigned*)(src + (8 * s + i) * 8192);
    const int sw = (r >> 1) & 7;
    const LDS_AS char* wa = lds + r * 128;
#pragma unroll
    for (int s = 0; s < 28; ++s) {
      if ((s & 7) == 0) asm volatile("s_barrier" ::: "memory");
      const LDS_AS char* p = wa + (s >> 2) * 8192 + (((2 * (s & 3) + h) ^ sw) * 16);
      acc0 = MFMA(ldsr(p), asb(tk[s]), acc0); acc1 = MFMA(ldsr(p + 4096), asb(tk[s]), acc1);
    }
    const int p = 32 * tt + r, l2 = (p >> 2) & 63, mg = p >> 8;
    tok = 64 * (l2 & 31) + 32 * (mg >> 2) + 8 * (mg & 3) + 4 * (l2 >> 5) + (p & 3); colb = 64 * nt;
  }
  char* dst = (char*)out + tok * 1792 + colb * 2 + h * 16;
#pragma unroll
  for (int nb = 0; nb < 2; ++nb) {
    const f32x16& a = nb ? acc1 : acc0;
#pragma unroll
    for (int pr = 0; pr < 2; ++pr) {
      const unsigned a0 = pk(a[8 * pr], a[8 * pr + 1]), a1 = pk(a[8 * pr + 2], a[8 * pr + 3]), b0 = pk(a[8 * pr + 4], a[8 * pr + 5]), b1 = pk(a[8 * pr + 6], a[8 * pr + 7]);
      auto x = __builtin_amdgcn_permlane32_swap(a0, b0, false, false);
      auto y = __builtin_amdgcn_permlane32_swap(a1, b1, false, false);
      *(u32x4*)(dst + nb * 64 + pr * 32) = (u32x4){x[0], y[0], x[1], y[1]};
    }
  }
}

extern "C" void kernel_launch(void* const* d_in, const int* in_sizes, int n_in, void* d_out, int out_size, void* d_ws, size_t ws_size, hipStream_t stream) {
  char* ws = (char*)d_ws;
  k1_proj<<<256, 512, 0, stream>>>((const u16*)d_in[0], (const u16*)d_in[1], (const u16*)d_in[2], (const u16*)d_in[3], (const u16*)d_in[4], ws);
  k2_attn<<<256, 320, 0, stream>>>(ws, ws);
  k3_out<<<224, 512, 0, stream>>>(ws, (const u16*)d_in[5], (u16*)d_out);
}
```

```cpp
#include <hip/hip_runtime.h>
#include <stdint.h>

typedef unsigned short u16;
typedef short bf16x8 __attribute__((ext_vector_type(8)));
typedef short s16x4 __attribute__((ext_vector_type(4)));
typedef float f32x16 __attribute__((ext_vector_type(16)));
typedef float f32x4 __attribute__((ext_vector_type(4)));
typedef float f32x2 __attribute__((ext_vector_type(2)));
typedef unsigned u32x4 __attribute__((ext_vector_type(4)));
typedef unsigned u32x2 __attribute__((ext_vector_type(2)));
typedef __bf16 bf16x2_t __attribute__((ext_vector_type(2)));
#define LDS_AS __attribute__((address_space(3)))
#define DI __device__ __forceinline__
#define MFMA(a, b, c) __builtin_amdgcn_mfma_f32_32x32x16_bf16((a), (b), (c), 0, 0, 0)
#define SBAR() __builtin_amdgcn_sched_barrier(0)
#define PIN(x) asm volatile("" : "+v"(x))

constexpr int SEQ = 2048, DM = 448, NH = 16, DF = 225;

namespace ct {
constexpr double PI = 3.14159265358979323846264338327950288;
constexpr double tsin(double x) { double x2 = x * x, t = x, s = x; for (int i = 1; i < 12; ++i) { t *= -x2 / ((2 * i) * (2 * i + 1)); s += t; } return s; }
constexpr double tcos(double x) { double x2 = x * x, t = 1, s = 1; for (int i = 1; i < 12; ++i) { t *= -x2 / ((2 * i - 1) * (2 * i)); s += t; } return s; }
constexpr double cos2pi(int j, int N) {
  j %= N; if (j < 0) j += N; if (2 * j > N) j = N - j; bool neg = false; if (4 * j > N) { j = N / 2 - j; neg = true; }
  double r = (8 * j > N) ? tsin(2 * PI * (N / 4 - j) / N) : tcos(2 * PI * j / N); return neg ? -r : r; }
constexpr double sin2pi(int j, int N) { return cos2pi(j - N / 4, N); }
constexpr u16 bf(double v) { float f = (float)v; unsigned u = __builtin_bit_cast(unsigned, f); u += 0x7fffu + ((u >> 16) & 1u); return (u16)(u >> 16); }
struct C448 { u16 c[448]; u16 ns[448]; };
constexpr C448 mk448() { C448 t{}; for (int j = 0; j < 448; ++j) { t.c[j] = bf(cos2pi(j, 448)); t.ns[j] = bf(-sin2pi(j, 448)); } return t; }
constexpr C448 T448 = mk448();
struct FPart { u16 v[56 * 448]; };
constexpr FPart mkF(int part) { FPart t{};
  for (int r = 0; r < 56; ++r) { int d = part * 56 + r; int a = 0; u16* o = &t.v[r * 448];
    for (int c = 0; c < 224; ++c) { o[2 * c] = T448.c[a]; o[2 * c + 1] = T448.ns[a]; a += d; if (a >= 448) a -= 448; }
    o[1] = (d & 1) ? 0xbf80 : 0x3f80; }
  return t; }
constexpr FPart F0 = mkF(0), F1 = mkF(1), F2 = mkF(2), F3 = mkF(3), F4 = mkF(4), F5 = mkF(5), F6 = mkF(6), F7 = mkF(7);
struct FTab { FPart p0, p1, p2, p3, p4, p5, p6, p7; u16 pad[128]; };
constexpr int kperm(int t, int h, int j) { return 16 * t + 8 * (j >> 2) + 4 * h + (j & 3); }
struct A2Tab {
  u16 t32f[3][2][64][8];
  u16 t64[3][2][4][64][8];
  u16 t32i[3][2][64][8];
};
constexpr A2Tab mkA2() { A2Tab t{};
  const double SC = 2.0 / (2048.0 * 448.0);
  for (int s = 0; s < 2; ++s) for (int l = 0; l < 64; ++l) for (int j = 0; j < 8; ++j) {
    int a = kperm(s, l >> 5, j) * (l & 31);
    t.t32f[0][s][l][j] = bf(cos2pi(a, 32)); t.t32f[1][s][l][j] = bf(sin2pi(a, 32)); t.t32f[2][s][l][j] = bf(-sin2pi(a, 32));
    int b = (16 * s + 8 * (l >> 5) + j) * (l & 31);
    t.t32i[0][s][l][j] = bf(SC * cos2pi(b, 32)); t.t32i[1][s][l][j] = bf(SC * sin2pi(b, 32)); t.t32i[2][s][l][j] = bf(-SC * sin2pi(b, 32)); }
  for (int m = 0; m < 2; ++m) for (int k = 0; k < 4; ++k) for (int l = 0; l < 64; ++l) for (int j = 0; j < 8; ++j) {
    int a = (32 * m + (l & 31)) * (32 * (k >> 1) + kperm(k & 1, l >> 5, j));
    t.t64[0][m][k][l][j] = bf(cos2pi(a, 64)); t.t64[1][m][k][l][j] = bf(sin2pi(a, 64)); t.t64[2][m][k][l][j] = bf(-sin2pi(a, 64)); }
  return t; }
struct TwTab { float tw[2][4][2][64][4]; };
constexpr TwTab mkTw() { TwTab t{};
  for (int m = 0; m < 2; ++m) for (int g = 0; g < 4; ++g) for (int l = 0; l < 64; ++l) for (int i = 0; i < 4; ++i) {
    int a = (32 * m + 8 * g + 4 * (l >> 5) + i) * (l & 31);
    t.tw[m][g][0][l][i] = (float)cos2pi(a, 2048); t.tw[m][g][1][l][i] = (float)sin2pi(a, 2048); }
  return t; }
}
__device__ __attribute__((aligned(256))) const ct::FTab g_F = { ct::F0, ct::F1, ct::F2, ct::F3, ct::F4, ct::F5, ct::F6, ct::F7, {} };
__device__ __attribute__((aligned(256))) const ct::A2Tab g_A2 = ct::mkA2();
__device__ __attribute__((aligned(256))) const ct::TwTab g_TW = ct::mkTw();

constexpr size_t WS_QF = 0;
constexpr size_t WS_KV = WS_QF + (2u << 20);
constexpr size_t WS_X1 = WS_KV + (4u << 20);
constexpr size_t WS_WT = WS_X1 + (2u << 20);
constexpr size_t WS_OF = WS_WT + (2u << 20);
constexpr size_t WS_Z2 = WS_OF + (2u << 20);

#define STAMPC(wsp, k, s, v) do {} while (0)
#define CYC() 0ull
#define STAMPT(wsp, k, s) do {} while (0)
DI unsigned pk(float a, float b) { f32x2 v = {a, b}; return __builtin_bit_cast(unsigned, __builtin_convertvector(v, bf16x2_t)); }
DI u32x4 pk8(const f32x16& x, int s) { u32x4 r; r[0] = pk(x[8 * s], x[8 * s + 1]); r[1] = pk(x[8 * s + 2], x[8 * s + 3]); r[2] = pk(x[8 * s + 4], x[8 * s + 5]); r[3] = pk(x[8 * s + 6], x[8 * s + 7]); return r; }
DI bf16x8 asb(u32x4 v) { return __builtin_bit_cast(bf16x8, v); }
DI bf16x8 ldsr(const LDS_AS char* p) { return *(const LDS_AS bf16x8*)p; }
DI s16x4 ldtr(const LDS_AS char* p) { return __builtin_amdgcn_ds_read_tr16_b64_v4i16((LDS_AS s16x4*)p); }
DI bf16x8 ldtr2(const LDS_AS char* p, int off2) { s16x4 lo = ldtr(p), hi = ldtr(p + off2); return __builtin_shufflevector(lo, hi, 0, 1, 2, 3, 4, 5, 6, 7); }
DI void dma16(const char* g, LDS_AS char* l) { __builtin_amdgcn_global_load_lds((const __attribute__((address_space(1))) unsigned*)g, (LDS_AS unsigned*)l, 16, 0, 0); }
#define WAITV(n) asm volatile("s_waitcnt vmcnt(" #n ")" ::: "memory")
#define BARRIER() asm volatile("s_waitcnt lgkmcnt(0)\n\ts_barrier" ::: "memory")
DI const f32x16 zero16() { f32x16 z; for (int i = 0; i < 16; ++i) z[i] = 0.f; return z; }

template <bool TRANS>
DI void k1_loop(const LDS_AS char* lds, int lane, int wm, int wn, f32x16 (&acc)[2][2], char* ws) {
  const int r = lane & 31, h = lane >> 5;
  const int sw = (r >> 1) & 7;
  const LDS_AS char* xa = lds + (wm * 64 + r) * 128;
  int xo[4];
#pragma unroll
  for (int s = 0; s < 4; ++s) xo[s] = ((2 * s + h) ^ sw) * 16;
  const int q = (lane & 15) >> 2;
  const LDS_AS char* wb0 = lds + 16384 + (8 * h + q) * 256 + (((2 * wn) ^ q) * 64) + ((lane >> 4) & 1) * 32 + (lane & 3) * 8;
  const LDS_AS char* wb1 = lds + 16384 + (8 * h + q) * 256 + (((2 * wn + 1) ^ q) * 64) + ((lane >> 4) & 1) * 32 + (lane & 3) * 8;
  bf16x8 a0, a1, b0, b1, na0, na1, nb0, nb1;
  PIN(acc[0][0]); PIN(acc[0][1]); PIN(acc[1][0]); PIN(acc[1][1]);
  unsigned long long tt[9]; tt[0] = CYC();
  BARRIER();
  tt[1] = CYC();
  a0 = ldsr(xa + xo[0]); a1 = ldsr(xa + xo[0] + 4096); b0 = ldtr2(wb0, 1024); b1 = ldtr2(wb1, 1024);
  int so = 0;
#define K1_MF(i, j, A, B) acc[i][j] = TRANS ? MFMA(B, A, acc[i][j]) : MFMA(A, B, acc[i][j])
#pragma unroll 1
  for (int c = 0; c < 7; ++c) {
#pragma unroll
    for (int s = 0; s < 4; ++s) {
      int xoff, woff;
      if (s < 3) { xoff = so + xo[s + 1]; woff = so + (s + 1) * 4096; }
      else {
        if (c < 6) BARRIER();
        tt[c + 2] = CYC();
        so = (so == 4 * 32768) ? 0 : so + 32768;
        xoff = so + xo[0]; woff = so;
      }
      SBAR();
      K1_MF(0, 0, a0, b0); SBAR();
      na0 = ldsr(xa + xoff); nb0 = ldtr2(wb0 + woff, 1024); SBAR();
      K1_MF(0, 1, a0, b1); SBAR();
      nb1 = ldtr2(wb1 + woff, 1024); SBAR();
      K1_MF(1, 0, a1, b0); SBAR();
      na1 = ldsr(xa + xoff + 4096); SBAR();
      K1_MF(1, 1, a1, b1); SBAR();
      a0 = na0; a1 = na1; b0 = nb0; b1 = nb1;
    }
  }
  if (wm + wn == 0) { STAMPC(ws, 0, 1, (tt[1] - tt[0]) * 100 + (1ull << 62)); STAMPC(ws, 0, 2, (tt[2] - tt[0]) * 100 + (1ull << 62)); STAMPC(ws, 0, 5, (tt[4] - tt[0]) * 100 + (1ull << 62)); STAMPC(ws, 0, 7, (tt[7] - tt[0]) * 100 + (1ull << 62));
 STAMPC(ws, 2, 3, (tt[3] - tt[0]) * 100 + (1ull << 62)); STAMPC(ws, 2, 4, (tt[5] - tt[0]) * 100 + (1ull << 62)); STAMPC(ws, 2, 5, (tt[6] - tt[0]) * 100 + (1ull << 62)); STAMPC(ws, 2, 6, (tt[8] - tt[0]) * 100 + (1ull << 62)); }
}

__global__ __launch_bounds__(512, 2) void k1_proj(const u16* __restrict__ ax, const u16* __restrict__ wr, const u16* __restrict__ wi,
                                                  const u16* __restrict__ bx, const u16* __restrict__ wqkv, char* __restrict__ ws) {
  __shared__ __attribute__((aligned(1024))) char smem[5 * 32768];
  LDS_AS char* lds = (LDS_AS char*)smem;
  const int bid = blockIdx.x, xcd = bid & 7, jj = bid >> 3;
  const int mt = 8 * (xcd & 1) + (jj & 7), nt = 4 * (xcd >> 1) + (jj >> 3), type = nt >> 2;
  const int lane = threadIdx.x & 63, wave = __builtin_amdgcn_readfirstlane(threadIdx.x >> 6);
  if (wave >= 4) {
    const int lw = wave - 4;
    unsigned long long lt0 = CYC();
    unsigned wv[2][8]; int wc[2], wf1[2];
    { const int a = bid >> 4, o = (bid >> 2) & 3, cg = bid & 3;
#pragma unroll
      for (int k = 0; k < 2; ++k) {
        int u = lw * 64 + lane + 256 * k; u = u > 455 ? 455 : u;
        const int f1l = (u * 1150) >> 16, cl = u - 57 * f1l;
        int c = 57 * cg + cl; c = c > 224 ? 224 : c;
        wc[k] = c; wf1[k] = 8 * o + f1l;
        const int off = ((128 * a + 8 * o + f1l) * DF + c) * 2;
#pragma unroll
        for (int i = 0; i < 4; ++i) {
          asm volatile("global_load_ushort %0, %1, %2" : "=v"(wv[k][2 * i]) : "v"(off + i * 32 * DF * 2), "s"(wr));
          asm volatile("global_load_ushort %0, %1, %2" : "=v"(wv[k][2 * i + 1]) : "v"(off + i * 32 * DF * 2), "s"(wi));
        }
      } }
    const char* xb = (const char*)(type == 3 ? ax : bx);
    const char* wbase = type == 3 ? (const char*)&g_F + (nt - 12) * 256 : (const char*)wqkv + nt * 224;
    const int ldb = type == 3 ? 896 : 2688, blk = type == 3 ? 64 : 56, last = type == 3 ? 48 : 40;
    unsigned vx[4], vw[4];
#pragma unroll
    for (int i = 0; i < 4; ++i) {
      const int p = lw * 4 + i;
      const int row = 8 * p + (lane >> 3), kg = (lane & 7) ^ ((row >> 1) & 7);
      const int tok = type == 3 ? 64 * (row & 31) + 4 * mt + (row >> 5) : 128 * mt + row;
      vx[i] = tok * 896 + kg * 16;
      const int wrow = 4 * p + (lane >> 4), b = ((lane >> 2) & 3) ^ (wrow & 3), pi = lane & 3;
      vw[i] = wrow * ldb + b * blk + (pi == 3 ? last : pi * 16);
    }
    auto issue = [&](int c, int slot) {
      const char* xs = xb + c * 128; const char* wsrc = wbase + c * 64 * ldb;
      LDS_AS char* l = lds + slot * 32768 + lw * 4096;
#pragma unroll
      for (int i = 0; i < 4; ++i) dma16(xs + vx[i], l + i * 1024);
#pragma unroll
      for (int i = 0; i < 4; ++i) dma16(wsrc + vw[i], l + 16384 + i * 1024);
    };
    SBAR(); unsigned long long lt1 = CYC(); SBAR();
#pragma unroll
    for (int c = 0; c < 5; ++c) issue(c, c);
    SBAR(); unsigned long long lt2 = CYC(); SBAR();
    asm volatile("s_waitcnt vmcnt(32)\n\ts_barrier"
                 : "+v"(wv[0][0]), "+v"(wv[0][1]), "+v"(wv[0][2]), "+v"(wv[0][3]), "+v"(wv[0][4]), "+v"(wv[0][5]), "+v"(wv[0][6]), "+v"(wv[0][7]),
                   "+v"(wv[1][0]), "+v"(wv[1][1]), "+v"(wv[1][2]), "+v"(wv[1][3]), "+v"(wv[1][4]), "+v"(wv[1][5]), "+v"(wv[1][6]), "+v"(wv[1][7]) :: "memory");
    { const int a = bid >> 4;
#pragma unroll
      for (int k = 0; k < 2; ++k) {
        u32x4 o;
        const bool half = (wc[k] == 0) || (wc[k] == 224);
#pragma unroll
        for (int i = 0; i < 4; ++i) {
          unsigned re = wv[k][2 * i] << 16, im = wv[k][2 * i + 1] << 16;
          if (half) { re = __float_as_uint(__uint_as_float(re) * 0.5f); im = __float_as_uint(__uint_as_float(im) * 0.5f); }
          o[i] = (re >> 16) | (im & 0xffff0000u);
        }
        *(u32x4*)(ws + WS_WT + wc[k] * 8192 + ((a >> 1) * 64 + wf1[k] + 32 * (a & 1)) * 16) = o;
      } }
    asm volatile("s_waitcnt vmcnt(26)\n\ts_barrier" ::: "memory");
    issue(5, 0);
    asm volatile("s_waitcnt vmcnt(26)\n\ts_barrier" ::: "memory");
    issue(6, 1);
    asm volatile("s_waitcnt vmcnt(26)\n\ts_barrier" ::: "memory");
    asm volatile("s_waitcnt vmcnt(18)\n\ts_barrier" ::: "memory");
    asm volatile("s_waitcnt vmcnt(8)\n\ts_barrier" ::: "memory");
    asm volatile("s_waitcnt vmcnt(0)\n\ts_barrier" ::: "memory");
    return;
  }
  const int wm = wave & 1, wn = wave >> 1, r = lane & 31, h = lane >> 5;
  if (wave == 0) STAMPT(ws, 0, 0);
  unsigned long long cy0 = CYC();
  f32x16 acc[2][2];
#pragma unroll
  for (int i = 0; i < 2; ++i)
#pragma unroll
    for (int j = 0; j < 2; ++j) acc[i][j] = zero16();
  if (type < 2) k1_loop<true>(lds, lane, wm, wn, acc, ws); else k1_loop<false>(lds, lane, wm, wn, acc, ws);
  { unsigned long long cy1 = CYC(); if (wave == 1) STAMPC(ws, 0, 6, (cy1 - cy0) * 100 + (1ull << 62)); }
  if (wave == 0) STAMPT(ws, 0, 3);
#pragma unroll
  for (int mi = 0; mi < 2; ++mi)
#pragma unroll
    for (int ni = 0; ni < 2; ++ni) {
      f32x16 v = acc[mi][ni];
      const int blk = 2 * wn + ni, tt = 4 * mt + 2 * wm + mi, head = 4 * (nt & 3) + blk;
      u32x4 p0, p1; char* dst;
      if (type == 0) {
        const float qs = 0.18898223650461362f * 1.4426950408889634f;
#pragma unroll
        for (int i = 0; i < 16; ++i) v[i] *= qs;
        p0 = pk8(v, 0); p1 = pk8(v, 1);
        if (h == 0) { p1[2] = 0; p1[3] = 0; }
        dst = ws + WS_QF + (head * 64 + tt) * 2048;
      } else if (type == 1) {
        p0 = pk8(v, 0); p1 = pk8(v, 1);
        if (h == 0) { p1[2] = 0x3f80u; p1[3] = 0; }
        dst = ws + WS_KV + (head * 64 + tt) * 4096;
      } else if (type == 2) {
        p0 = pk8(v, 0); p1 = pk8(v, 1);
        if ((r >> 2) == 6) { const unsigned c = r == 24 ? 0x3f803f80u : 0u; p0 = (u32x4){c, c, c, c}; p1 = p0; }
        dst = ws + WS_KV + (head * 64 + tt) * 4096 + 2048;
      } else {
        p0 = pk8(v, 0); p1 = pk8(v, 1);
        const int j = 4 * (nt - 12) + blk;
        if (j >= 14) continue;
        dst = ws + WS_X1 + (j * 64 + tt) * 2048;
      }
      *(u32x4*)(dst + lane * 16) = p0; *(u32x4*)(dst + 1024 + lane * 16) = p1;
    }
  if (wave == 0) STAMPT(ws, 0, 4);
}

DI float swapmax(float m) { auto rr = __builtin_amdgcn_permlane32_swap(__float_as_uint(m), __float_as_uint(m), false, false); return fmaxf(__uint_as_float(rr[0]), __uint_as_float(rr[1])); }
DI float max16(const f32x16& s) {
  float a = fmaxf(fmaxf(s[0], s[1]), s[2]), b = fmaxf(fmaxf(s[3], s[4]), s[5]);
  a = fmaxf(fmaxf(a, s[6]), s[7]); b = fmaxf(fmaxf(b, s[8]), s[9]); a = fmaxf(fmaxf(a, s[10]), s[11]); b = fmaxf(fmaxf(b, s[12]), s[13]);
  a = fmaxf(fmaxf(a, s[14]), s[15]); return fmaxf(a, b); }
#define EX2(x) __builtin_amdgcn_exp2f(x)

DI void a2_column(int c, int lane, const char* __restrict__ ws_in, char* __restrict__ ws, LDS_AS char* tl) {
  const int r = lane & 31, h = lane >> 5;
  const bool real_col = (c == 0) || (c == 224);
  const int cp = c == 224 ? 1 : 2 * c;
  const char* xsrc = ws_in + WS_X1 + ((cp >> 5) * 64 + r) * 2048 + ((cp & 31) + 32 * h) * 16;
  const char* tb = (const char*)&g_A2 + lane * 16;
  const char* twb = (const char*)&g_TW + lane * 16;
#define T32F(mat, t) (*(const bf16x8*)(tb + ((mat) * 2 + (t)) * 1024))
#define T64(mat, m, k) (*(const bf16x8*)(tb + 6144 + (((mat) * 2 + (m)) * 4 + (k)) * 1024))
#define T32I(mat, t) (*(const bf16x8*)(tb + 6144 + 24576 + ((mat) * 2 + (t)) * 1024))
#define TW(m, g, p) (*(const f32x4*)(twb + (((m) * 4 + (g)) * 2 + (p)) * 1024))
  const char* tb2 = tb; asm volatile("" : "+v"(tb2));
  const char* twb2 = twb; asm volatile("" : "+v"(twb2));
#define T64V(mat, m, k) (*(const bf16x8*)(tb2 + 6144 + (((mat) * 2 + (m)) * 4 + (k)) * 1024))
#define TWV(m, g, p) (*(const f32x4*)(twb2 + (((m) * 4 + (g)) * 2 + (p)) * 1024))
  bf16x8 ur[2][2], ui[2][2], t32[3][2], t64[3][2][4], t64b[3][4]; f32x4 tw[2][4][2]; u32x4 wv[2][4];
#pragma unroll
  for (int m = 0; m < 2; ++m)
#pragma unroll
    for (int t = 0; t < 2; ++t) {
      ur[m][t] = *(const bf16x8*)(xsrc + m * 65536 + t * 1024);
      ui[m][t] = *(const bf16x8*)(xsrc + m * 65536 + t * 1024 + (real_col ? 0 : 16));
    }
#pragma unroll
  for (int i = 0; i < 6; ++i) t32[i >> 1][i & 1] = T32F(i >> 1, i & 1);
#pragma unroll
  for (int i = 0; i < 16; ++i) tw[i >> 3][(i >> 1) & 3][i & 1] = TW(i >> 3, (i >> 1) & 3, i & 1);
  SBAR();
  if (real_col) {
#pragma unroll
    for (int m = 0; m < 2; ++m)
#pragma unroll
      for (int t = 0; t < 2; ++t) ui[m][t] = (bf16x8){0, 0, 0, 0, 0, 0, 0, 0};
  }
  f32x16 re[2], im[2];
#pragma unroll
  for (int m = 0; m < 2; ++m) {
    re[m] = MFMA(ur[m][0], t32[0][0], zero16()); im[m] = MFMA(ui[m][0], t32[0][0], zero16());
    re[m] = MFMA(ur[m][1], t32[0][1], re[m]);    im[m] = MFMA(ui[m][1], t32[0][1], im[m]);
    re[m] = MFMA(ui[m][0], t32[1][0], re[m]);    im[m] = MFMA(ur[m][0], t32[2][0], im[m]);
    re[m] = MFMA(ui[m][1], t32[1][1], re[m]);    im[m] = MFMA(ur[m][1], t32[2][1], im[m]);
  }
  SBAR();
#pragma unroll
  for (int i = 0; i < 12; ++i) t64[i >> 2][0][i & 3] = T64(i >> 2, 0, i & 3);
  SBAR();
  u32x4 br[4], bi[4];
#pragma unroll
  for (int m = 0; m < 2; ++m) {
#pragma unroll
    for (int g = 0; g < 4; ++g) { const f32x4 cs = tw[m][g][0], sn = tw[m][g][1];
#pragma unroll
      for (int i = 0; i < 4; ++i) { const float a = re[m][4 * g + i], b = im[m][4 * g + i];
        re[m][4 * g + i] = a * cs[i] + b * sn[i]; im[m][4 * g + i] = b * cs[i] - a * sn[i]; } }
    br[2 * m] = pk8(re[m], 0); br[2 * m + 1] = pk8(re[m], 1); bi[2 * m] = pk8(im[m], 0); bi[2 * m + 1] = pk8(im[m], 1);
  }
  SBAR();
#pragma unroll
  for (int i = 0; i < 12; ++i) t64[i >> 2][1][i & 3] = T64(i >> 2, 1, i & 3);
  { const char* wt = ws_in + WS_WT + c * 8192 + lane * 16;
#pragma unroll
    for (int i = 0; i < 8; ++i) wv[i >> 2][i & 3] = *(const u32x4*)(wt + i * 1024); }
  SBAR();
#pragma unroll
  for (int m = 0; m < 2; ++m) {
    re[m] = zero16(); im[m] = zero16();
#pragma unroll
    for (int k = 0; k < 4; ++k) {
      re[m] = MFMA(t64[0][m][k], asb(br[k]), re[m]); im[m] = MFMA(t64[0][m][k], asb(bi[k]), im[m]);
      re[m] = MFMA(t64[1][m][k], asb(bi[k]), re[m]); im[m] = MFMA(t64[2][m][k], asb(br[k]), im[m]);
    }
  }
  SBAR();
#pragma unroll
  for (int i = 0; i < 12; ++i) t64b[i >> 2][i & 3] = T64V(i >> 2, 0, i & 3);
  SBAR();
#pragma unroll
  for (int m = 0; m < 2; ++m) {
#pragma unroll
    for (int g = 0; g < 4; ++g) { const u32x4 w = wv[m][g];
#pragma unroll
      for (int i = 0; i < 4; ++i) { const float a = re[m][4 * g + i], b = im[m][4 * g + i];
        const float wre = __uint_as_float(w[i] << 16), wim = __uint_as_float(w[i] & 0xffff0000u);
        re[m][4 * g + i] = a * wre - b * wim; im[m][4 * g + i] = a * wim + b * wre; } }
    br[2 * m] = pk8(re[m], 0); br[2 * m + 1] = pk8(re[m], 1); bi[2 * m] = pk8(im[m], 0); bi[2 * m + 1] = pk8(im[m], 1);
  }
  SBAR();
#pragma unroll
  for (int i = 0; i < 8; ++i) tw[0][i >> 1][i & 1] = TWV(0, i >> 1, i & 1);
  SBAR();
#pragma unroll
  for (int m = 0; m < 2; ++m) {
    re[m] = zero16(); im[m] = zero16();
#pragma unroll
    for (int k = 0; k < 4; ++k) {
      const bf16x8 C = m ? t64[0][1][k] : t64b[0][k], S = m ? t64[1][1][k] : t64b[1][k], N = m ? t64[2][1][k] : t64b[2][k];
      re[m] = MFMA(C, asb(br[k]), re[m]); im[m] = MFMA(C, asb(bi[k]), im[m]);
      re[m] = MFMA(N, asb(bi[k]), re[m]); im[m] = MFMA(S, asb(br[k]), im[m]);
    }
    SBAR();
    if (m == 0) {
#pragma unroll
      for (int i = 0; i < 8; ++i) tw[1][i >> 1][i & 1] = TWV(1, i >> 1, i & 1);
      SBAR();
    }
  }
#pragma unroll
  for (int i = 0; i < 6; ++i) t32[i >> 1][i & 1] = T32I(i >> 1, i & 1);
  SBAR();
#pragma unroll
  for (int m = 0; m < 2; ++m)
#pragma unroll
    for (int g = 0; g < 4; ++g) { const f32x4 cs = tw[m][g][0], sn = tw[m][g][1];
      float xr[4], xi[4];
#pragma unroll
      for (int i = 0; i < 4; ++i) { const float a = re[m][4 * g + i], b = im[m][4 * g + i];
        xr[i] = a * cs[i] - b * sn[i]; xi[i] = b * cs[i] + a * sn[i]; }
      LDS_AS char* p = tl + r * 192 + (32 * m + 8 * g + 4 * h) * 2;
      *(LDS_AS u32x2*)p = (u32x2){pk(xr[0], xr[1]), pk(xr[2], xr[3])};
      *(LDS_AS u32x2*)(p + 6144) = (u32x2){pk(xi[0], xi[1]), pk(xi[2], xi[3])};
    }
  asm volatile("s_waitcnt lgkmcnt(0)" ::: "memory");
  const LDS_AS char* tp = tl + (8 * h + ((lane & 15) >> 2)) * 192 + ((lane >> 4) & 1) * 32 + (lane & 3) * 8;
#pragma unroll
  for (int m = 0; m < 2; ++m) {
    re[m] = zero16(); im[m] = zero16();
#pragma unroll
    for (int t = 0; t < 2; ++t) {
      const bf16x8 ar = ldtr2(tp + t * 16 * 192 + m * 64, 4 * 192), ai = ldtr2(tp + 6144 + t * 16 * 192 + m * 64, 4 * 192);
      re[m] = MFMA(ar, t32[0][t], re[m]); im[m] = MFMA(ai, t32[0][t], im[m]);
      re[m] = MFMA(ai, t32[2][t], re[m]); im[m] = MFMA(ar, t32[1][t], im[m]);
    }
  }
  if (!real_col) {
    char* z = ws + WS_Z2 + c * 8192 + lane * 16;
#pragma unroll
    for (int m = 0; m < 2; ++m)
#pragma unroll
      for (int g = 0; g < 4; ++g)
        *(u32x4*)(z + (m * 4 + g) * 1024) = (u32x4){pk(re[m][4 * g], im[m][4 * g]), pk(re[m][4 * g + 1], im[m][4 * g + 1]), pk(re[m][4 * g + 2], im[m][4 * g + 2]), pk(re[m][4 * g + 3], im[m][4 * g + 3])};
  } else {
    char* z = ws + WS_Z2 + lane * 16 + (c ? 2 : 0);
#pragma unroll
    for (int m = 0; m < 2; ++m)
#pragma unroll
      for (int g = 0; g < 16; ++g) *(u16*)(z + (m * 4 + (g >> 2)) * 1024 + (g & 3) * 4) = (u16)pk(re[m][g], 0.f);
  }
}

constexpr int K2_EX = 0, K2_MX = 65536, K2_CNT = K2_MX + 2048, K2_TL = K2_CNT + 64, K2_LDS = K2_TL + 12288;

#define A_MF(d, a, b, c) "v_mfma_f32_32x32x16_bf16 v[" #d ":" #d "+15], v[" #a ":" #a "+3], v[" #b ":" #b "+3], " c "\n\t"
#define A_ACC(d) "v[" #d ":" #d "+15]"
#define A_EXP(s, p) "v_exp_f32 v168, v[" #s "]\n\tv_exp_f32 v169, v[" #s "+1]\n\tv_exp_f32 v170, v[" #s "+2]\n\tv_exp_f32 v171, v[" #s "+3]\n\t" \
                    "v_cvt_pk_bf16_f32 v[" #p "], v168, v169\n\tv_cvt_pk_bf16_f32 v[" #p "+1], v170, v171\n\t"
#define A_PERIOD(SC, SN, KN, QN, OC, V0C, OP, V1P, W3) \
  A_MF(SN, KN, QN, "0")                 A_EXP(SC, 160) \
  A_MF(OP, V1P, 164, A_ACC(OP))         A_EXP(SC + 4, 162) \
  A_MF(SN, KN + 4, QN + 4, A_ACC(SN))   A_EXP(SC + 8, 164) \
  W3 A_MF(OC, V0C, 160, A_ACC(OC))      A_EXP(SC + 12, 166)
#define A_ADDR(idx) "s_add_i32 s40, " idx ", %[rot]\n\ts_and_b32 s40, s40, 63\n\ts_lshl_b32 s40, s40, 12\n\ts_add_u32 s42, %[kvlo], s40\n\ts_addc_u32 s43, %[kvhi], 0\n\t"
#define A_TICKET "s_mov_b64 exec, 1\n\tds_add_rtn_u32 v175, v173, v174\n\ts_mov_b64 exec, -1\n\t"
#define A_HALF(L, KC, VC, KO, VO, IO, IN)   \
  A_TICKET \
  A_PERIOD(64, 80, KC, 104, 0, VC, 48, VO + 4, "s_waitcnt vmcnt(2)\n\t") \
  "s_cmp_ge_i32 " IO ", 64\n\ts_cbranch_scc1 " L "a%=\n\t" A_ADDR(IO) \
  "global_load_dwordx4 v[" #VO ":" #VO "+3], v172, s[42:43] offset:2048\n\tglobal_load_dwordx4 v[" #VO "+4:" #VO "+7], v172, s[42:43] offset:3072\n" \
  L "a%=:\n\t" \
  A_PERIOD(80, 64, KC, 112, 16, VC, 0, VC + 4, "") \
  A_PERIOD(64, 80, KC, 120, 32, VC, 16, VC + 4, "") \
  "s_waitcnt lgkmcnt(0)\n\tv_readfirstlane_b32 " IN ", v175\n\ts_nop 0\n\ts_cmp_ge_i32 " IN ", 64\n\ts_cbranch_scc1 " L "b%=\n\t" A_ADDR(IN) \
  "global_load_dwordx4 v[" #KC ":" #KC "+3], v172, s[42:43]\n\tglobal_load_dwordx4 v[" #KC "+4:" #KC "+7], v172, s[42:43] offset:1024\n\t" \
  "s_waitcnt vmcnt(4)\n\ts_branch " L "c%=\n" \
  L "b%=:\n\ts_waitcnt vmcnt(0)\n" \
  L "c%=:\n\t" \
  A_PERIOD(80, 64, KO, 96, 48, VC, 32, VC + 4, "")

__global__ __launch_bounds__(320, 2) void k2_attn(const char* __restrict__ wsi, char* __restrict__ ws) {
  __shared__ __attribute__((aligned(1024))) char smem[K2_LDS];
  LDS_AS char* lds = (LDS_AS char*)smem;
  const int bid = blockIdx.x, xcd = bid & 7, jj = bid >> 3;
  const int head = 2 * xcd + (jj & 1), qb = jj >> 1;
  const int lane = threadIdx.x & 63, wave = __builtin_amdgcn_readfirstlane(threadIdx.x >> 6);
  const int h = lane >> 5;
  if (wave == 4) {
    __builtin_amdgcn_s_setprio(2);
    if (lane == 0) *(LDS_AS int*)(lds + K2_CNT) = 8;
    BARRIER();
    const int c = (jj >> 2) * 32 + xcd * 4 + (jj & 3);
    if (c < DF) a2_column(c, lane, wsi, ws, lds + K2_TL);
    STAMPT(ws, 1, 5);
    return;
  }
  const char* qsrc = wsi + WS_QF + (head * 64 + 4 * qb) * 2048 + lane * 16;
  const char* kvb = wsi + WS_KV + head * 64 * 4096 + lane * 16;
  const int rot = 4 * qb;
  bf16x8 KA0, KA1, VA0, VA1, KB0, KB1;
#define LDK(K0, K1, i) do { const char* p_ = kvb + ((((i) + rot) & 63) << 12); K0 = *(const bf16x8*)p_; K1 = *(const bf16x8*)(p_ + 1024); } while (0)
#define LDV(V0, V1, i) do { const char* p_ = kvb + ((((i) + rot) & 63) << 12); V0 = *(const bf16x8*)(p_ + 2048); V1 = *(const bf16x8*)(p_ + 3072); } while (0)
  if (wave == 0) STAMPT(ws, 1, 0);
  LDK(KA0, KA1, wave);
  bf16x8 Q0[4], Q1[4];
#pragma unroll
  for (int t = 0; t < 4; ++t) { Q0[t] = *(const bf16x8*)(qsrc + t * 2048); Q1[t] = *(const bf16x8*)(qsrc + t * 2048 + 1024); }
  LDV(VA0, VA1, wave); LDK(KB0, KB1, wave + 4);
  BARRIER();
  f32x16 O[4]; float mref[4];
#pragma unroll
  for (int t = 0; t < 4; ++t) {
    f32x16 s = MFMA(KA0, Q0[t], zero16()); s = MFMA(KA1, Q1[t], s);
    const float m = swapmax(max16(s));
    const unsigned nb = pk(-m, 0.f);
    mref[t] = -__uint_as_float(nb << 16);
    u32x4 q = __builtin_bit_cast(u32x4, Q1[t]); q[2] = h == 0 ? nb : q[2]; Q1[t] = asb(q);
    O[t] = zero16();
  }
  if (wave == 0) STAMPT(ws, 1, 1);
  unsigned long long cy0 = CYC();
  {
    f32x16 SA = MFMA(KA0, Q0[0], zero16()); SA = MFMA(KA1, Q1[0], SA);
    const uint64_t kvs = (uint64_t)(wsi + WS_KV + head * 64 * 4096);
    const unsigned kvlo = (unsigned)kvs, kvhi = (unsigned)(kvs >> 32);
    int ib = wave + 4, ic;
    u32x4 VB1 = {0, 0, 0, 0}, P1 = {0, 0, 0, 0};
    asm volatile(
      "s_nop 7\n"
      "Ltop%=:\n\t"
      A_HALF("La", 128, 136, 144, 152, "%[ib]", "%[ic]")
      "s_cmp_ge_i32 %[ib], 64\n\ts_cbranch_scc1 LendA%=\n\t"
      A_HALF("Lb", 144, 152, 128, 136, "%[ic]", "%[ib]")
      "s_cmp_lt_i32 %[ic], 64\n\ts_cbranch_scc1 Ltop%=\n\t"
      A_MF(48, 156, 164, A_ACC(48))
      "s_branch Ldone%=\n"
      "LendA%=:\n\t"
      A_MF(48, 140, 164, A_ACC(48))
      "Ldone%=:\n\t"
      "s_nop 11"
      : "+{v[0:15]}"(O[0]), "+{v[16:31]}"(O[1]), "+{v[32:47]}"(O[2]), "+{v[48:63]}"(O[3]), "+{v[64:79]}"(SA),
        "+{v[128:131]}"(KA0), "+{v[132:135]}"(KA1), "+{v[136:139]}"(VA0), "+{v[140:143]}"(VA1), "+{v[144:147]}"(KB0), "+{v[148:151]}"(KB1),
        "+{v[156:159]}"(VB1), "+{v[164:167]}"(P1), [ib] "+s"(ib), [ic] "=&s"(ic)
      : "{v[96:99]}"(Q0[0]), "{v[100:103]}"(Q1[0]), "{v[104:107]}"(Q0[1]), "{v[108:111]}"(Q1[1]), "{v[112:115]}"(Q0[2]), "{v[116:119]}"(Q1[2]), "{v[120:123]}"(Q0[3]), "{v[124:127]}"(Q1[3]),
        "{v172}"(lane * 16), "{v173}"((unsigned)(uintptr_t)(lds + K2_CNT)), "{v174}"(1),
        [rot] "s"(rot), [kvlo] "s"(kvlo), [kvhi] "s"(kvhi)
      : "memory", "scc", "s40", "s42", "s43", "v80", "v81", "v82", "v83", "v84", "v85", "v86", "v87", "v88", "v89", "v90", "v91", "v92", "v93", "v94", "v95",
        "v152", "v153", "v154", "v155", "v160", "v161", "v162", "v163", "v168", "v169", "v170", "v171", "v175");
  }
  { unsigned long long cy1 = CYC(); if (wave == 1) STAMPC(ws, 1, 6, (cy1 - cy0) * 100 + (1ull << 62)); }
  if (wave == 0) STAMPT(ws, 1, 2);
  if (wave == 1) STAMPT(ws, 1, 3);
#pragma unroll
  for (int t = 0; t < 4; ++t) {
    if (t != wave) {
      LDS_AS char* e = lds + K2_EX + (wave * 4 + t) * 4096 + lane * 16;
#pragma unroll
      for (int g = 0; g < 4; ++g) *(LDS_AS f32x4*)(e + g * 1024) = (f32x4){O[t][4 * g], O[t][4 * g + 1], O[t][4 * g + 2], O[t][4 * g + 3]};
      *(LDS_AS float*)(lds + K2_MX + (wave * 4 + t) * 128 + (lane & 31) * 4) = mref[t];
    }
  }
  f32x16 acc; float mo;
  if (wave == 0) { acc = O[0]; mo = mref[0]; } else if (wave == 1) { acc = O[1]; mo = mref[1]; } else if (wave == 2) { acc = O[2]; mo = mref[2]; } else { acc = O[3]; mo = mref[3]; }
  BARRIER();
  {
    float mw[3]; float M = mo;
#pragma unroll
    for (int k = 0; k < 3; ++k) { const int w = (wave + 1 + k) & 3; mw[k] = *(const LDS_AS float*)(lds + K2_MX + (w * 4 + wave) * 128 + (lane & 31) * 4); M = fmaxf(M, mw[k]); }
    bool ok = (acc[12] >= 0x1p-40f && acc[12] <= 0x1p40f);
    const float fo = EX2(mo - M);
#pragma unroll
    for (int i = 0; i < 16; ++i) acc[i] *= fo;
#pragma unroll
    for (int k = 0; k < 3; ++k) { const int w = (wave + 1 + k) & 3; const float f = EX2(mw[k] - M);
      const LDS_AS char* e = lds + K2_EX + (w * 4 + wave) * 4096 + lane * 16;
#pragma unroll
      for (int g = 0; g < 4; ++g) { const f32x4 v = *(const LDS_AS f32x4*)(e + g * 1024);
        if (g == 3) ok = ok && (v[0] >= 0x1p-40f && v[0] <= 0x1p40f);
#pragma unroll
        for (int i = 0; i < 4; ++i) acc[4 * g + i] = fmaf(v[i], f, acc[4 * g + i]); } }
    ok = ok || h == 1;
    float am = 0.f;
#pragma unroll
    for (int i = 0; i < 16; i += 2) am = fmaxf(fmaxf(am, fabsf(acc[i])), fabsf(acc[i + 1]));
    float sm = 0.f;
#pragma unroll
    for (int i = 0; i < 16; ++i) sm += acc[i] * 0.f;
    ok = ok && (sm == 0.f);
    (void)am;
    if (__builtin_expect(!__all(ok), 0)) {
      bf16x8 q0 = *(const bf16x8*)(qsrc + wave * 2048), q1 = *(const bf16x8*)(qsrc + wave * 2048 + 1024);
      float m = -INFINITY; acc = zero16();
      for (int kt = 0; kt < 64; ++kt) {
        const char* p = kvb + (kt << 12);
        f32x16 s = MFMA(*(const bf16x8*)p, q0, zero16()); s = MFMA(*(const bf16x8*)(p + 1024), q1, s);
        const float mn = fmaxf(m, swapmax(max16(s)));
        const float al = EX2(m - mn); m = mn;
#pragma unroll
        for (int i = 0; i < 16; ++i) { acc[i] *= al; s[i] = EX2(s[i] - mn); }
        acc = MFMA(*(const bf16x8*)(p + 2048), asb(pk8(s, 0)), acc); acc = MFMA(*(const bf16x8*)(p + 3072), asb(pk8(s, 1)), acc);
      }
    }
    auto rr = __builtin_amdgcn_permlane32_swap(__float_as_uint(acc[12]), __float_as_uint(acc[12]), false, false);
    const float inv = 1.0f / __uint_as_float(rr[0]);
#pragma unroll
    for (int i = 0; i < 16; ++i) acc[i] *= inv;
    u32x4 p0 = pk8(acc, 0), p1 = pk8(acc, 1);
    if (h == 0) { p1[2] = 0; p1[3] = 0; }
    char* dst = ws + WS_OF + ((4 * qb + wave) * 16 + head) * 2048 + lane * 16;
    *(u32x4*)dst = p0; *(u32x4*)(dst + 1024) = p1;
  }
  if (wave == 0) STAMPT(ws, 1, 4);
}

__global__ __launch_bounds__(512, 2) void k3_out(const char* __restrict__ ws, const u16* __restrict__ wo, u16* __restrict__ out) {
  __shared__ __attribute__((aligned(1024))) char smem[57344];
  LDS_AS char* lds = (LDS_AS char*)smem;
  const int bid = blockIdx.x, xcd = bid & 7, jj = bid >> 3;
  const int part = xcd & 1, mt = 4 * (xcd >> 1) + (jj & 3), nt = jj >> 2;
  const int lane = threadIdx.x & 63, wave = __builtin_amdgcn_readfirstlane(threadIdx.x >> 6);
  if (wave >= 4) {
    const int lw = wave - 4, rr = lane >> 3, sl = lane & 7;
    const char* src; int pstride;
    if (part) { src = (const char*)wo + nt * 128 + rr * 896 + (((sl >> 2) ^ ((rr >> 1) & 1)) * 64) + (sl & 3) * 16 + lw * 8 * 896; pstride = 32 * 896; }
    else { src = nullptr; pstride = 0; }
#pragma unroll
    for (int i = 0; i < 14; ++i) {
      const int p = lw + 4 * i;
      const char* g;
      if (part) g = src + i * pstride;
      else { const int seg = p >> 3, row = 8 * (p & 7) + rr; g = (const char*)&g_F + (64 * nt + row) * 896 + seg * 128 + ((sl ^ ((row >> 1) & 7)) * 16); }
      dma16(g, lds + p * 1024);
    }
    asm volatile("s_waitcnt vmcnt(10)\n\ts_barrier" ::: "memory");
    asm volatile("s_waitcnt vmcnt(6)\n\ts_barrier" ::: "memory");
    asm volatile("s_waitcnt vmcnt(2)\n\ts_barrier" ::: "memory");
    asm volatile("s_waitcnt vmcnt(0)\n\ts_barrier" ::: "memory");
    return;
  }
  const int r = lane & 31, h = lane >> 5, tt = 4 * mt + wave;
  if (wave == 0) STAMPT(ws, 2, 0);
  f32x16 acc0 = zero16(), acc1 = zero16();
  int tok, colb;
  if (part) {
    bf16x8 tk[32];
    const char* src = ws + WS_OF + tt * 32768 + lane * 16;
#pragma unroll
    for (int s = 0; s < 32; ++s) tk[s] = *(const bf16x8*)(src + s * 1024);
    const int q = (lane & 15) >> 2;
    const LDS_AS char* w0 = lds + (4 * h + q) * 128 + ((q >> 1) * 64) + ((lane >> 4) & 1) * 32 + (lane & 3) * 8;
    const LDS_AS char* w1 = lds + (4 * h + q) * 128 + (((q >> 1) ^ 1) * 64) + ((lane >> 4) & 1) * 32 + (lane & 3) * 8;
#pragma unroll
    for (int s = 0; s < 32; ++s) {
      if ((s & 7) == 0) asm volatile("s_barrier" ::: "memory");
      const int o = (s >> 1) * 3584 + (s & 1) * 2048, o2 = (s & 1) ? 512 : 1024;
      acc0 = MFMA(ldtr2(w0 + o, o2), tk[s], acc0); acc1 = MFMA(ldtr2(w1 + o, o2), tk[s], acc1);
    }
    tok = 32 * tt + r; colb = 448 + 64 * nt;
  } else {
    u32x4 tk[28];
    const char* src = ws + WS_Z2 + h * 4 * 8192 + (32 * tt + r) * 4;
#pragma unroll
    for (int s = 0; s < 28; ++s)
#pragma unroll
      for (int i = 0; i < 4; ++i) tk[s][i] = *(const unsigned*)(src + (8 * s + i) * 8192);
    const int sw = (r >> 1) & 7;
    const LDS_AS char* wa = lds + r * 128;
#pragma unroll
    for (int s = 0; s < 28; ++s) {
      if ((s & 7) == 0) asm volatile("s_barrier" ::: "memory");
      const LDS_AS char* p = wa + (s >> 2) * 8192 + (((2 * (s & 3) + h) ^ sw) * 16);
      acc0 = MFMA(ldsr(p), asb(tk[s]), acc0); acc1 = MFMA(ldsr(p + 4096), asb(tk[s]), acc1);
    }
    const int p = 32 * tt + r, l2 = (p >> 2) & 63, mg = p >> 8;
    tok = 64 * (l2 & 31) + 32 * (mg >> 2) + 8 * (mg & 3) + 4 * (l2 >> 5) + (p & 3); colb = 64 * nt;
  }
  if (wave == 0) STAMPT(ws, 2, 1);
  char* dst = (char*)out + tok * 1792 + colb * 2 + h * 16;
#pragma unroll
  for (int nb = 0; nb < 2; ++nb) {
    const f32x16& a = nb ? acc1 : acc0;
#pragma unroll
    for (int pr = 0; pr < 2; ++pr) {
      const unsigned a0 = pk(a[8 * pr], a[8 * pr + 1]), a1 = pk(a[8 * pr + 2], a[8 * pr + 3]), b0 = pk(a[8 * pr + 4], a[8 * pr + 5]), b1 = pk(a[8 * pr + 6], a[8 * pr + 7]);
      auto x = __builtin_amdgcn_permlane32_swap(a0, b0, false, false);
      auto y = __builtin_amdgcn_permlane32_swap(a1, b1, false, false);
      *(u32x4*)(dst + nb * 64 + pr * 32) = (u32x4){x[0], y[0], x[1], y[1]};
    }
  }
  if (wave == 0) STAMPT(ws, 2, 2);
}

extern "C" void kernel_launch(void* const* d_in, const int* in_sizes, int n_in, void* d_out, int out_size, void* d_ws, size_t ws_size, hipStream_t stream) {
  char* ws = (char*)d_ws;
  k1_proj<<<256, 512, 0, stream>>>((const u16*)d_in[0], (const u16*)d_in[1], (const u16*)d_in[2], (const u16*)d_in[3], (const u16*)d_in[4], ws);
  k2_attn<<<256, 320, 0, stream>>>(ws, ws);
  k3_out<<<224, 512, 0, stream>>>(ws, (const u16*)d_in[5], (u16*)d_out);
}
```

```cpp
#include <hip/hip_runtime.h>
#include <stdint.h>

typedef unsigned short u16;
typedef short bf16x8 __attribute__((ext_vector_type(8)));
typedef short s16x4 __attribute__((ext_vector_type(4)));
typedef float f32x16 __attribute__((ext_vector_type(16)));
typedef float f32x4 __attribute__((ext_vector_type(4)));
typedef float f32x2 __attribute__((ext_vector_type(2)));
typedef unsigned u32x4 __attribute__((ext_vector_type(4)));
typedef unsigned u32x2 __attribute__((ext_vector_type(2)));
typedef __bf16 bf16x2_t __attribute__((ext_vector_type(2)));
#define LDS_AS __attribute__((address_space(3)))
#define DI __device__ __forceinline__
#define MFMA(a, b, c) __builtin_amdgcn_mfma_f32_32x32x16_bf16((a), (b), (c), 0, 0, 0)
#define SBAR() __builtin_amdgcn_sched_barrier(0)
#define PIN(x) asm volatile("" : "+v"(x))

constexpr int DF = 225;
#define K2_N0 13
#define K1_ROT 1
#define K1_PF 1
#define PRIO_M 0
#define PRIO_V 2
#define A2_SLEEP 8

namespace ct {
constexpr double PI = 3.14159265358979323846264338327950288;
constexpr double tsin(double x) { double x2 = x * x, t = x, s = x; for (int i = 1; i < 12; ++i) { t *= -x2 / ((2 * i) * (2 * i + 1)); s += t; } return s; }
constexpr double tcos(double x) { double x2 = x * x, t = 1, s = 1; for (int i = 1; i < 12; ++i) { t *= -x2 / ((2 * i - 1) * (2 * i)); s += t; } return s; }
constexpr double cos2pi(int j, int N) {
  j %= N; if (j < 0) j += N; if (2 * j > N) j = N - j; bool neg = false; if (4 * j > N) { j = N / 2 - j; neg = true; }
  double r = (8 * j > N) ? tsin(2 * PI * (N / 4 - j) / N) : tcos(2 * PI * j / N); return neg ? -r : r; }
constexpr double sin2pi(int j, int N) { return cos2pi(j - N / 4, N); }
constexpr u16 bf(double v) { float f = (float)v; unsigned u = __builtin_bit_cast(unsigned, f); u += 0x7fffu + ((u >> 16) & 1u); return (u16)(u >> 16); }
struct C448 { u16 c[448]; u16 ns[448]; };
constexpr C448 mk448() { C448 t{}; for (int j = 0; j < 448; ++j) { t.c[j] = bf(cos2pi(j, 448)); t.ns[j] = bf(-sin2pi(j, 448)); } return t; }
constexpr C448 T448 = mk448();
struct FPart { u16 v[56 * 448]; };
constexpr FPart mkF(int part) { FPart t{};
  for (int r = 0; r < 56; ++r) { int d = part * 56 + r; int a = 0; u16* o = &t.v[r * 448];
    for (int c = 0; c < 224; ++c) { o[2 * c] = T448.c[a]; o[2 * c + 1] = T448.ns[a]; a += d; if (a >= 448) a -= 448; }
    o[1] = (d & 1) ? 0xbf80 : 0x3f80; }
  return t; }
constexpr FPart F0 = mkF(0), F1 = mkF(1), F2 = mkF(2), F3 = mkF(3), F4 = mkF(4), F5 = mkF(5), F6 = mkF(6), F7 = mkF(7);
struct FTab { FPart p0, p1, p2, p3, p4, p5, p6, p7; u16 pad[128]; };
constexpr int kperm(int t, int h, int j) { return 16 * t + 8 * (j >> 2) + 4 * h + (j & 3); }
struct A2Tab {
  u16 t32f[3][2][64][8];
  u16 t64[3][2][4][64][8];
  u16 t32i[3][2][64][8];
};
constexpr A2Tab mkA2() { A2Tab t{};
  const double SC = 2.0 / (2048.0 * 448.0);
  for (int s = 0; s < 2; ++s) for (int l = 0; l < 64; ++l) for (int j = 0; j < 8; ++j) {
    int a = kperm(s, l >> 5, j) * (l & 31);
    t.t32f[0][s][l][j] = bf(cos2pi(a, 32)); t.t32f[1][s][l][j] = bf(sin2pi(a, 32)); t.t32f[2][s][l][j] = bf(-sin2pi(a, 32));
    int b = (16 * s + 8 * (l >> 5) + j) * (l & 31);
    t.t32i[0][s][l][j] = bf(SC * cos2pi(b, 32)); t.t32i[1][s][l][j] = bf(SC * sin2pi(b, 32)); t.t32i[2][s][l][j] = bf(-SC * sin2pi(b, 32)); }
  for (int m = 0; m < 2; ++m) for (int k = 0; k < 4; ++k) for (int l = 0; l < 64; ++l) for (int j = 0; j < 8; ++j) {
    int a = (32 * m + (l & 31)) * (32 * (k >> 1) + kperm(k & 1, l >> 5, j));
    t.t64[0][m][k][l][j] = bf(cos2pi(a, 64)); t.t64[1][m][k][l][j] = bf(sin2pi(a, 64)); t.t64[2][m][k][l][j] = bf(-sin2pi(a, 64)); }
  return t; }
struct TwTab { float tw[2][4][2][64][4]; };
constexpr TwTab mkTw() { TwTab t{};
  for (int m = 0; m < 2; ++m) for (int g = 0; g < 4; ++g) for (int l = 0; l < 64; ++l) for (int i = 0; i < 4; ++i) {
    int a = (32 * m + 8 * g + 4 * (l >> 5) + i) * (l & 31);
    t.tw[m][g][0][l][i] = (float)cos2pi(a, 2048); t.tw[m][g][1][l][i] = (float)sin2pi(a, 2048); }
  return t; }
}
__device__ __attribute__((aligned(256))) const ct::FTab g_F = { ct::F0, ct::F1, ct::F2, ct::F3, ct::F4, ct::F5, ct::F6, ct::F7, {} };
__device__ __attribute__((aligned(256))) const ct::A2Tab g_A2 = ct::mkA2();
__device__ __attribute__((aligned(256))) const ct::TwTab g_TW = ct::mkTw();

constexpr size_t WS_QF = 0;
constexpr size_t WS_KV = WS_QF + (2u << 20);
constexpr size_t WS_X1 = WS_KV + (4u << 20);
constexpr size_t WS_WT = WS_X1 + (2u << 20);
constexpr size_t WS_OF = WS_WT + (2u << 20);
constexpr size_t WS_Z2 = WS_OF + (2u << 20);

#define STAMP_DECL
#define ST(s) do {} while (0)
#define SC(s, base) do {} while (0)
#define CYC() 0ull
#define STAMP_FLUSH(wsp, k) do {} while (0)
DI unsigned pk(float a, float b) { f32x2 v = {a, b}; return __builtin_bit_cast(unsigned, __builtin_convertvector(v, bf16x2_t)); }
DI u32x4 pk8(const f32x16& x, int s) { u32x4 r; r[0] = pk(x[8 * s], x[8 * s + 1]); r[1] = pk(x[8 * s + 2], x[8 * s + 3]); r[2] = pk(x[8 * s + 4], x[8 * s + 5]); r[3] = pk(x[8 * s + 6], x[8 * s + 7]); return r; }
DI bf16x8 asb(u32x4 v) { return __builtin_bit_cast(bf16x8, v); }
DI bf16x8 ldsr(const LDS_AS char* p) { return *(const LDS_AS bf16x8*)p; }
DI s16x4 ldtr(const LDS_AS char* p) { return __builtin_amdgcn_ds_read_tr16_b64_v4i16((LDS_AS s16x4*)p); }
DI bf16x8 ldtr2(const LDS_AS char* p, int off2) { s16x4 lo = ldtr(p), hi = ldtr(p + off2); return __builtin_shufflevector(lo, hi, 0, 1, 2, 3, 4, 5, 6, 7); }
DI void dma16(const char* g, LDS_AS char* l) { __builtin_amdgcn_global_load_lds((const __attribute__((address_space(1))) unsigned*)g, (LDS_AS unsigned*)l, 16, 0, 0); }
#define WAITV(n) asm volatile("s_waitcnt vmcnt(" #n ")" ::: "memory")
#define BARRIER() asm volatile("s_waitcnt lgkmcnt(0)\n\ts_barrier" ::: "memory")
DI const f32x16 zero16() { f32x16 z; for (int i = 0; i < 16; ++i) z[i] = 0.f; return z; }

typedef __amdgpu_buffer_rsrc_t rsrc_t;
DI rsrc_t mkrsrc(const void* p, unsigned bytes) { return __builtin_amdgcn_make_buffer_rsrc((void*)p, 0, bytes, 0x00020000); }
DI u32x4 bld(rsrc_t r, int voff, int soff) { return __builtin_amdgcn_raw_buffer_load_b128(r, voff, soff, 0); }

constexpr int K1_SLOT = 30720;
template <bool TRANS, int WROW>
DI void k1_loop(const LDS_AS char* lds, int lane, int wm, int wn, f32x16 (&acc)[2][2], unsigned long long* sto) {
  constexpr int SLOT = 16384 + 64 * WROW, BLK = WROW / 4;
  const int r = lane & 31, h = lane >> 5;
  const int sw = (r >> 1) & 7;
  const LDS_AS char* xa = lds + (wm * 64 + r) * 128;
  int xo[4];
#pragma unroll
  for (int s = 0; s < 4; ++s) xo[s] = ((2 * s + h) ^ sw) * 16;
  const int q = (lane & 15) >> 2, c4 = ((lane >> 4) & 1) * 4 + (lane & 3);
  const LDS_AS char *wb0, *wb1; int t1;
  if (WROW == 224) {
    wb0 = lds + 16384 + (8 * h + 2 * q) * 224 + 2 * wn * 56 + (c4 >= 6 ? c4 - 1 : c4) * 8; wb1 = wb0 + 56; t1 = 224;
  } else {
    wb0 = lds + 16384 + (8 * h + q) * 256 + (((2 * wn) ^ q) * 64) + c4 * 8; wb1 = lds + 16384 + (8 * h + q) * 256 + (((2 * wn + 1) ^ q) * 64) + c4 * 8; t1 = 1024;
  }
  bf16x8 a0, a1, b0, b1, na0, na1, nb0, nb1;
  PIN(acc[0][0]); PIN(acc[0][1]); PIN(acc[1][0]); PIN(acc[1][1]);
  STAMP_DECL; unsigned long long c0 = CYC();
  BARRIER();
  SC(0, c0);
  a0 = ldsr(xa + xo[0]); b0 = ldtr2(wb0, t1); b1 = ldtr2(wb1, t1); a1 = ldsr(xa + xo[0] + 4096);
  int so = 0;
#define K1_MF(i, j, A, B) acc[i][j] = TRANS ? MFMA(B, A, acc[i][j]) : MFMA(A, B, acc[i][j])
#pragma unroll 1
  for (int c = 0; c < 7; ++c) {
#pragma unroll
    for (int s = 0; s < 4; ++s) {
      int xoff, woff;
      if (s < 3) { xoff = so + xo[s + 1]; woff = so + (s + 1) * 16 * WROW; }
      else {
        if (c < 6) BARRIER();
        SC(c + 1, c0);
        so = (so == 3 * SLOT) ? 0 : so + SLOT;
        xoff = so + xo[0]; woff = so;
      }
      SBAR();
      K1_MF(0, 0, a0, b0); SBAR();
      na0 = ldsr(xa + xoff); nb0 = ldtr2(wb0 + woff, t1); SBAR();
      K1_MF(0, 1, a0, b1); SBAR();
      nb1 = ldtr2(wb1 + woff, t1); na1 = ldsr(xa + xoff + 4096); SBAR();
      K1_MF(1, 0, a1, b0); SBAR();
      K1_MF(1, 1, a1, b1); SBAR();
      a0 = na0; a1 = na1; b0 = nb0; b1 = nb1;
    }
  }
}

template <int NW>
DI void k1_loader(LDS_AS char* lds, int lane, int lw, int bid, int mt, int nt, const u16* ax, const u16* wr, const u16* wi, const u16* bx, const u16* wqkv, char* ws) {
  const bool spec = nt >= 12;
  const char* xb = (const char*)(spec ? ax : bx);
  const char* wbase = spec ? (const char*)&g_F + (nt - 12) * 256 : (const char*)wqkv + nt * 224;
  const int ldb = spec ? 896 : 2688, slot = spec ? 32768 : 30720;
  unsigned vx[4], vw[4];
  int wp0;
#pragma unroll
  for (int i = 0; i < 4; ++i) {
    const int row = 8 * (lw * 4 + i) + (lane >> 3), kg = (lane & 7) ^ ((row >> 1) & 7);
    const int tok = spec ? 64 * (row & 31) + 4 * mt + (row >> 5) : 128 * mt + row;
    vx[i] = tok * 896 + kg * 16;
  }
  if (spec) {
    wp0 = lw * 4;
#pragma unroll
    for (int i = 0; i < 4; ++i) { const int wrow = 4 * (wp0 + i) + (lane >> 4); vw[i] = wrow * 896 + (((lane >> 2) & 3) ^ (wrow & 3)) * 64 + (lane & 3) * 16; }
  } else {
    wp0 = lw < 2 ? lw * 4 : 8 + (lw - 2) * 3;
#pragma unroll
    for (int i = 0; i < NW; ++i) { const int idx = 64 * (wp0 + i) + lane, pos = (idx * 4682) >> 16, pc = idx - 14 * pos;
      const int k = (pos & ~7) | ((pos & 1) << 2) | ((pos >> 1) & 3); vw[i] = k * 2688 + pc * 16; }
  }
  const int rot = K1_ROT ? (2 * (nt & 3) + (mt & 7)) % 7 : 0;
  auto issue = [&](int c) {
    const int cs = c + rot >= 7 ? c + rot - 7 : c + rot;
    const char* xs = xb + cs * 128; const char* wsrc = wbase + cs * 64 * ldb;
    LDS_AS char* l = lds + (c & 3) * slot;
#pragma unroll
    for (int i = 0; i < 4; ++i) dma16(xs + vx[i], l + (lw * 4 + i) * 1024);
#pragma unroll
    for (int i = 0; i < NW; ++i) dma16(wsrc + vw[i], l + 16384 + (wp0 + i) * 1024);
  };
  issue(0); issue(1); issue(2);
  unsigned wv[2][8]; int wc[2], wf1[2];
  const int a = bid >> 4;
  { const int o = (bid >> 2) & 3, cg = bid & 3;
#pragma unroll
    for (int k = 0; k < 2; ++k) {
      int u = lw * 64 + lane + 256 * k; u = u > 455 ? 455 : u;
      const int f1l = (u * 1150) >> 16, cl = u - 57 * f1l;
      int c = 57 * cg + cl; c = c > 224 ? 224 : c;
      wc[k] = c; wf1[k] = 8 * o + f1l;
      const int off = ((128 * a + 8 * o + f1l) * DF + c) * 2;
#pragma unroll
      for (int i = 0; i < 4; ++i) {
        asm volatile("global_load_ushort %0, %1, %2" : "=v"(wv[k][2 * i]) : "v"(off + i * 32 * DF * 2), "s"(wr));
        asm volatile("global_load_ushort %0, %1, %2" : "=v"(wv[k][2 * i + 1]) : "v"(off + i * 32 * DF * 2), "s"(wi));
      }
    } }
  constexpr int P = 4 + NW;
#define K1_WB(n) asm volatile("s_waitcnt vmcnt(%0)\n\ts_barrier" :: "i"(n) : "memory")
  K1_WB(2 * P + 16);
  issue(3);
  K1_WB(2 * P + 16);
  issue(4);
  K1_WB(2 * P + 16);
  issue(5);
  asm volatile("s_waitcnt vmcnt(%16)\n\ts_barrier"
               : "+v"(wv[0][0]), "+v"(wv[0][1]), "+v"(wv[0][2]), "+v"(wv[0][3]), "+v"(wv[0][4]), "+v"(wv[0][5]), "+v"(wv[0][6]), "+v"(wv[0][7]),
                 "+v"(wv[1][0]), "+v"(wv[1][1]), "+v"(wv[1][2]), "+v"(wv[1][3]), "+v"(wv[1][4]), "+v"(wv[1][5]), "+v"(wv[1][6]), "+v"(wv[1][7]) : "i"(2 * P) : "memory");
  issue(6);
  K1_WB(2 * P);
  K1_WB(P);
  K1_WB(0);
#pragma unroll
  for (int k = 0; k < 2; ++k) {
    u32x4 o;
    const bool half = (wc[k] == 0) || (wc[k] == 224);
#pragma unroll
    for (int i = 0; i < 4; ++i) {
      unsigned re = wv[k][2 * i] << 16, im = wv[k][2 * i + 1] << 16;
      if (half) { re = __float_as_uint(__uint_as_float(re) * 0.5f); im = __float_as_uint(__uint_as_float(im) * 0.5f); }
      o[i] = (re >> 16) | (im & 0xffff0000u);
    }
    *(u32x4*)(ws + WS_WT + wc[k] * 8192 + ((a >> 1) * 64 + wf1[k] + 32 * (a & 1)) * 16) = o;
  }
}

__global__ __launch_bounds__(512, 2) void k1_proj(const u16* __restrict__ ax, const u16* __restrict__ wr, const u16* __restrict__ wi,
                                                  const u16* __restrict__ bx, const u16* __restrict__ wqkv, char* __restrict__ ws) {
  __shared__ __attribute__((aligned(1024))) char smem[4 * 32768];
  LDS_AS char* lds = (LDS_AS char*)smem;
  const int bid = blockIdx.x, xcd = bid & 7, jj = bid >> 3;
  const int mt = 8 * (xcd & 1) + (jj & 7), nt = 4 * (xcd >> 1) + (jj >> 3), type = nt >> 2;
  const int lane = threadIdx.x & 63, wave = __builtin_amdgcn_readfirstlane(threadIdx.x >> 6);
  if (wave >= 4) {
    if (type == 3 || wave < 6) k1_loader<4>(lds, lane, wave - 4, bid, mt, nt, ax, wr, wi, bx, wqkv, ws);
    else k1_loader<3>(lds, lane, wave - 4, bid, mt, nt, ax, wr, wi, bx, wqkv, ws);
    return;
  }
  const int wm = wave & 1, wn = wave >> 1, r = lane & 31, h = lane >> 5;
  STAMP_DECL; unsigned long long sto[8]; ST(0);
  unsigned pf0 = 0, pf1 = 0;
  if (K1_PF) {
    int i = wave * 64 + lane; i = i > 223 ? 223 : i;
    const int L = (nt & 3) * 224 + i, row = (L * 9363) >> 16, seg = L - 7 * row;
    const int tok = type == 3 ? 64 * (row & 31) + 4 * mt + (row >> 5) : 128 * mt + row;
    asm volatile("global_load_dword %0, %1, %2" : "=v"(pf0) : "v"(tok * 896 + seg * 128), "s"(type == 3 ? ax : bx));
    int j = wave * 64 + lane; j = j > 167 ? 167 : j;
    const int wrow = (j * 21846) >> 16, ls = j - 3 * wrow;
    const int woff = (56 * (mt & 7) + wrow) * (type == 3 ? 896 : 2688) + (ls == 2 ? 220 : ls * 128);
    asm volatile("global_load_dword %0, %1, %2" : "=v"(pf1) : "v"(woff), "s"(type == 3 ? (const char*)&g_F + (nt - 12) * 256 : (const char*)wqkv + nt * 224));
  }
  f32x16 acc[2][2];
#pragma unroll
  for (int i = 0; i < 2; ++i)
#pragma unroll
    for (int j = 0; j < 2; ++j) acc[i][j] = zero16();
  if (type < 2) k1_loop<true, 224>(lds, lane, wm, wn, acc, sto); else if (type == 2) k1_loop<false, 224>(lds, lane, wm, wn, acc, sto); else k1_loop<false, 256>(lds, lane, wm, wn, acc, sto);
  asm volatile("s_waitcnt vmcnt(0)" : "+v"(pf0), "+v"(pf1));
  ST(1);
  const float qs = type == 0 ? 0.18898223650461362f * 1.4426950408889634f : 1.0f;
  const bool padqk = type < 2 && h == 0, padv = type == 2 && (r >> 2) == 6;
  const unsigned kone = type == 1 ? 0x3f80u : 0u, vone = r == 24 ? 0x3f803f80u : 0u;
  char* dst0 = type == 0 ? ws + WS_QF + (4 * nt * 64) * 2048 : type == 3 ? ws + WS_X1 + (4 * (nt - 12) * 64) * 2048 : ws + WS_KV + (4 * (nt & 3) * 64) * 4096 + (type == 2 ? 2048 : 0);
  const int tsz = (type == 0 || type == 3) ? 2048 : 4096;
  dst0 += (2 * wn * 64 + 4 * mt + 2 * wm) * tsz + lane * 16;
  if (nt == 15 && wn == 1) return;
#pragma unroll
  for (int mi = 0; mi < 2; ++mi)
#pragma unroll
    for (int ni = 0; ni < 2; ++ni) {
      f32x16 v = acc[mi][ni];
#pragma unroll
      for (int i = 0; i < 16; ++i) v[i] *= qs;
      u32x4 p0 = pk8(v, 0), p1 = pk8(v, 1);
      if (padqk) { p1[2] = kone; p1[3] = 0; }
      if (padv) { p0 = (u32x4){vone, vone, vone, vone}; p1 = p0; }
      char* dst = dst0 + (ni * 64 + mi) * tsz;
      *(u32x4*)dst = p0; *(u32x4*)(dst + 1024) = p1;
    }
  ST(2);
}

DI float swapmax(float m) { auto rr = __builtin_amdgcn_permlane32_swap(__float_as_uint(m), __float_as_uint(m), false, false); return fmaxf(__uint_as_float(rr[0]), __uint_as_float(rr[1])); }
DI float max16(const f32x16& s) {
  float a = fmaxf(fmaxf(s[0], s[1]), s[2]), b = fmaxf(fmaxf(s[3], s[4]), s[5]);
  a = fmaxf(fmaxf(a, s[6]), s[7]); b = fmaxf(fmaxf(b, s[8]), s[9]); a = fmaxf(fmaxf(a, s[10]), s[11]); b = fmaxf(fmaxf(b, s[12]), s[13]);
  a = fmaxf(fmaxf(a, s[14]), s[15]); return fmaxf(a, b); }
#define EX2(x) __builtin_amdgcn_exp2f(x)

template <bool CONJ>
DI void cmul2(float a0, float b0, float c0, float s0, float a1, float b1, float c1, float s1, unsigned& re, unsigned& im) {
  float t0, t1, t2, t3;
  if (CONJ)
    asm("v_mul_f32 %2, %7, %9\n\tv_mul_f32 %3, %6, %9\n\tv_mul_f32 %4, %11, %13\n\tv_mul_f32 %5, %10, %13\n\t"
        "v_fma_f32 %2, %6, %8, %2\n\tv_fma_f32 %3, %7, %8, -%3\n\tv_fma_f32 %4, %10, %12, %4\n\tv_fma_f32 %5, %11, %12, -%5\n\t"
        "v_cvt_pk_bf16_f32 %0, %2, %4\n\tv_cvt_pk_bf16_f32 %1, %3, %5"
        : "=v"(re), "=v"(im), "=&v"(t0), "=&v"(t1), "=&v"(t2), "=&v"(t3) : "v"(a0), "v"(b0), "v"(c0), "v"(s0), "v"(a1), "v"(b1), "v"(c1), "v"(s1));
  else
    asm("v_mul_f32 %2, %7, %9\n\tv_mul_f32 %3, %6, %9\n\tv_mul_f32 %4, %11, %13\n\tv_mul_f32 %5, %10, %13\n\t"
        "v_fma_f32 %2, %6, %8, -%2\n\tv_fma_f32 %3, %7, %8, %3\n\tv_fma_f32 %4, %10, %12, -%4\n\tv_fma_f32 %5, %11, %12, %5\n\t"
        "v_cvt_pk_bf16_f32 %0, %2, %4\n\tv_cvt_pk_bf16_f32 %1, %3, %5"
        : "=v"(re), "=v"(im), "=&v"(t0), "=&v"(t1), "=&v"(t2), "=&v"(t3) : "v"(a0), "v"(b0), "v"(c0), "v"(s0), "v"(a1), "v"(b1), "v"(c1), "v"(s1));
}
#define MF_DONE(x, y, z, w) do { __builtin_amdgcn_s_setprio(PRIO_V); asm volatile("s_nop 11" : "+v"(x), "+v"(y), "+v"(z), "+v"(w)); SBAR(); } while (0)
#define VA_DONE(b) do { __builtin_amdgcn_s_setprio(PRIO_M); asm volatile("s_nop 1" : "+v"(b[0]), "+v"(b[1]), "+v"(b[2]), "+v"(b[3]), "+v"(b[4]), "+v"(b[5]), "+v"(b[6]), "+v"(b[7])); SBAR(); } while (0)

DI void a2_column(int c, int lane, const char* __restrict__ ws_in, char* __restrict__ ws, LDS_AS char* tl) {
  const int r = lane & 31, h = lane >> 5, l16 = lane * 16;
  const bool real_col = (c == 0) || (c == 224);
  const int cp = c == 224 ? 1 : 2 * c;
  const rsrc_t rx = mkrsrc(ws_in + WS_X1 + (cp >> 5) * 131072 + (cp & 31) * 16, 0x40000u);
  const rsrc_t rt = mkrsrc(&g_A2, sizeof(g_A2)), rw = mkrsrc(&g_TW, sizeof(g_TW)), rf = mkrsrc(ws_in + WS_WT + c * 8192, 8192);
  const int xv = r * 2048 + h * 512;
#define T32F(mat, t) asb(bld(rt, l16, ((mat) * 2 + (t)) * 1024))
#define T64(mat, m, k) asb(bld(rt, l16, 6144 + (((mat) * 2 + (m)) * 4 + (k)) * 1024))
#define T32I(mat, t) asb(bld(rt, l16, 6144 + 24576 + ((mat) * 2 + (t)) * 1024))
#define TW(m, g, p) __builtin_bit_cast(f32x4, bld(rw, l16, (((m) * 4 + (g)) * 2 + (p)) * 1024))
  bf16x8 ur[2][2], ui[2][2], t32[3][2], t64[3][2][4]; f32x4 tw[2][4][2]; u32x4 wv[2][4];
#pragma unroll
  for (int m = 0; m < 2; ++m)
#pragma unroll
    for (int t = 0; t < 2; ++t) { ur[m][t] = asb(bld(rx, xv, m * 65536 + t * 1024)); ui[m][t] = asb(bld(rx, xv, m * 65536 + t * 1024 + 16)); }
#pragma unroll
  for (int i = 0; i < 6; ++i) t32[i >> 1][i & 1] = T32F(i >> 1, i & 1);
#pragma unroll
  for (int i = 0; i < 16; ++i) tw[i >> 3][(i >> 1) & 3][i & 1] = TW(i >> 3, (i >> 1) & 3, i & 1);
  SBAR();
  if (real_col) {
#pragma unroll
    for (int m = 0; m < 2; ++m)
#pragma unroll
      for (int t = 0; t < 2; ++t) ui[m][t] = (bf16x8){0, 0, 0, 0, 0, 0, 0, 0};
  }
  __builtin_amdgcn_s_setprio(PRIO_M);
  f32x16 re[2], im[2];
#pragma unroll
  for (int m = 0; m < 2; ++m) {
    re[m] = MFMA(ur[m][0], t32[0][0], zero16()); im[m] = MFMA(ui[m][0], t32[0][0], zero16());
    re[m] = MFMA(ur[m][1], t32[0][1], re[m]);    im[m] = MFMA(ui[m][1], t32[0][1], im[m]);
    re[m] = MFMA(ui[m][0], t32[1][0], re[m]);    im[m] = MFMA(ur[m][0], t32[2][0], im[m]);
    re[m] = MFMA(ui[m][1], t32[1][1], re[m]);    im[m] = MFMA(ur[m][1], t32[2][1], im[m]);
  }
  SBAR();
#pragma unroll
  for (int i = 0; i < 12; ++i) t64[i >> 2][0][i & 3] = T64(i >> 2, 0, i & 3);
  MF_DONE(re[0], im[0], re[1], im[1]);
  u32x4 b[8];
#pragma unroll
  for (int m = 0; m < 2; ++m)
#pragma unroll
    for (int e = 0; e < 16; e += 2) { const f32x4 cs = tw[m][e >> 2][0], sn = tw[m][e >> 2][1]; unsigned pr, pi;
      cmul2<true>(re[m][e], im[m][e], cs[e & 3], sn[e & 3], re[m][e + 1], im[m][e + 1], cs[(e & 3) + 1], sn[(e & 3) + 1], pr, pi);
      b[2 * m + (e >> 3)][(e >> 1) & 3] = pr; b[4 + 2 * m + (e >> 3)][(e >> 1) & 3] = pi; }
  SBAR();
#pragma unroll
  for (int i = 0; i < 12; ++i) t64[i >> 2][1][i & 3] = T64(i >> 2, 1, i & 3);
#pragma unroll
  for (int i = 0; i < 8; ++i) wv[i >> 2][i & 3] = bld(rf, l16, i * 1024);
  VA_DONE(b);
#pragma unroll
  for (int m = 0; m < 2; ++m) {
    re[m] = zero16(); im[m] = zero16();
#pragma unroll
    for (int k = 0; k < 4; ++k) {
      re[m] = MFMA(t64[0][m][k], asb(b[k]), re[m]); im[m] = MFMA(t64[0][m][k], asb(b[4 + k]), im[m]);
      re[m] = MFMA(t64[1][m][k], asb(b[4 + k]), re[m]); im[m] = MFMA(t64[2][m][k], asb(b[k]), im[m]);
    }
  }
  SBAR();
  { int l2 = l16; asm volatile("" : "+v"(l2));
#pragma unroll
    for (int i = 0; i < 12; ++i) t64[i >> 2][0][i & 3] = asb(bld(rt, l2, 6144 + ((i >> 2) * 8 + (i & 3)) * 1024)); }
  MF_DONE(re[0], im[0], re[1], im[1]);
  int l3 = l16; asm volatile("" : "+v"(l3));
#pragma unroll
  for (int m = 0; m < 2; ++m)
#pragma unroll
    for (int e = 0; e < 16; e += 2) { const unsigned w0 = wv[m][e >> 2][e & 3], w1 = wv[m][e >> 2][(e & 3) + 1]; unsigned pr, pi;
      cmul2<false>(re[m][e], im[m][e], __uint_as_float(w0 << 16), __uint_as_float(w0 & 0xffff0000u), re[m][e + 1], im[m][e + 1], __uint_as_float(w1 << 16), __uint_as_float(w1 & 0xffff0000u), pr, pi);
      b[2 * m + (e >> 3)][(e >> 1) & 3] = pr; b[4 + 2 * m + (e >> 3)][(e >> 1) & 3] = pi; }
  SBAR();
#pragma unroll
  for (int i = 0; i < 8; ++i) tw[0][i >> 1][i & 1] = __builtin_bit_cast(f32x4, bld(rw, l3, i * 1024));
  VA_DONE(b);
#pragma unroll
  for (int m = 0; m < 2; ++m) {
    re[m] = zero16(); im[m] = zero16();
#pragma unroll
    for (int k = 0; k < 4; ++k) {
      re[m] = MFMA(t64[0][m][k], asb(b[k]), re[m]); im[m] = MFMA(t64[0][m][k], asb(b[4 + k]), im[m]);
      re[m] = MFMA(t64[2][m][k], asb(b[4 + k]), re[m]); im[m] = MFMA(t64[1][m][k], asb(b[k]), im[m]);
    }
    SBAR();
    if (m == 0) {
#pragma unroll
      for (int i = 0; i < 8; ++i) tw[1][i >> 1][i & 1] = __builtin_bit_cast(f32x4, bld(rw, l3, (8 + i) * 1024));
      SBAR();
    }
  }
#pragma unroll
  for (int i = 0; i < 6; ++i) t32[i >> 1][i & 1] = T32I(i >> 1, i & 1);
  MF_DONE(re[0], im[0], re[1], im[1]);
#pragma unroll
  for (int m = 0; m < 2; ++m)
#pragma unroll
    for (int g = 0; g < 4; ++g) { const f32x4 cs = tw[m][g][0], sn = tw[m][g][1]; u32x2 pr, pi; unsigned x, y;
      cmul2<false>(re[m][4 * g], im[m][4 * g], cs[0], sn[0], re[m][4 * g + 1], im[m][4 * g + 1], cs[1], sn[1], x, y); pr[0] = x; pi[0] = y;
      cmul2<false>(re[m][4 * g + 2], im[m][4 * g + 2], cs[2], sn[2], re[m][4 * g + 3], im[m][4 * g + 3], cs[3], sn[3], x, y); pr[1] = x; pi[1] = y;
      LDS_AS char* p = tl + r * 192 + (32 * m + 8 * g + 4 * h) * 2;
      *(LDS_AS u32x2*)p = pr; *(LDS_AS u32x2*)(p + 6144) = pi;
    }
  __builtin_amdgcn_s_setprio(PRIO_M);
  asm volatile("s_waitcnt lgkmcnt(0)" ::: "memory");
  const LDS_AS char* tp = tl + (8 * h + ((lane & 15) >> 2)) * 192 + ((lane >> 4) & 1) * 32 + (lane & 3) * 8;
#pragma unroll
  for (int m = 0; m < 2; ++m) {
    re[m] = zero16(); im[m] = zero16();
#pragma unroll
    for (int t = 0; t < 2; ++t) {
      const bf16x8 ar = ldtr2(tp + t * 16 * 192 + m * 64, 4 * 192), ai = ldtr2(tp + 6144 + t * 16 * 192 + m * 64, 4 * 192);
      re[m] = MFMA(ar, t32[0][t], re[m]); im[m] = MFMA(ai, t32[0][t], im[m]);
      re[m] = MFMA(ai, t32[2][t], re[m]); im[m] = MFMA(ar, t32[1][t], im[m]);
    }
  }
  SBAR(); __builtin_amdgcn_s_setprio(PRIO_V);
  if (!real_col) {
    char* z = ws + WS_Z2 + c * 8192 + lane * 16;
#pragma unroll
    for (int m = 0; m < 2; ++m)
#pragma unroll
      for (int g = 0; g < 4; ++g)
        *(u32x4*)(z + (m * 4 + g) * 1024) = (u32x4){pk(re[m][4 * g], im[m][4 * g]), pk(re[m][4 * g + 1], im[m][4 * g + 1]), pk(re[m][4 * g + 2], im[m][4 * g + 2]), pk(re[m][4 * g + 3], im[m][4 * g + 3])};
  } else {
    char* z = ws + WS_Z2 + lane * 16 + (c ? 2 : 0);
#pragma unroll
    for (int m = 0; m < 2; ++m)
#pragma unroll
      for (int g = 0; g < 16; ++g) *(u16*)(z + (m * 4 + (g >> 2)) * 1024 + (g & 3) * 4) = (u16)pk(re[m][g], 0.f);
  }
}

constexpr int K2_EX = 0, K2_MX = 49152, K2_TL = K2_MX + 3072, K2_LDS = K2_TL + 12288;

#define A_MF(d, a, b, c) "v_mfma_f32_32x32x16_bf16 v[" #d ":" #d "+15], v[" #a ":" #a "+3], v[" #b ":" #b "+3], " c "\n\t"
#define A_ACC(d) "v[" #d ":" #d "+15]"
#define A_EXP(s, p, X) X "v_exp_f32 v168, v[" #s "]\n\tv_exp_f32 v169, v[" #s "+1]\n\tv_exp_f32 v170, v[" #s "+2]\n\tv_exp_f32 v171, v[" #s "+3]\n\t" \
                    "v_cvt_pk_bf16_f32 v[" #p "], v168, v169\n\tv_cvt_pk_bf16_f32 v[" #p "+1], v170, v171\n\t"
#define A_PERIOD(SC, SN, KN, QN, OC, V0C, OP, V1P, XA, XB, XC, XD) \
  A_MF(SN, KN, QN, "0")                 A_EXP(SC, 160, XA) \
  A_MF(OP, V1P, 164, A_ACC(OP))         A_EXP(SC + 4, 162, XB) \
  A_MF(SN, KN + 4, QN + 4, A_ACC(SN))   A_EXP(SC + 8, 164, XC) \
  A_MF(OC, V0C, 160, A_ACC(OC))         A_EXP(SC + 12, 166, XD)
#define A_LD(d, so, off) "global_load_dwordx4 v[" #d ":" #d "+3], v172, " so " offset:" #off "\n\t"
#define A_HALF(KC, VC, KO, VO, PC, PC0, PC1, PO)   \
  A_PERIOD(64, 80, KC, 104, 0, VC, 48, VO + 4, "", A_LD(VO, PO, 2048) A_LD(VO + 4, PO, 3072), "s_waitcnt vmcnt(4)\n\t", "") \
  A_PERIOD(80, 64, KC, 112, 16, VC, 0, VC + 4, "s_add_u32 %[off], %[off], 0x1000\n\ts_and_b32 %[off], %[off], 0x3ffff\n\t", "s_add_u32 " PC0 ", %[kvlo], %[off]\n\ts_addc_u32 " PC1 ", %[kvhi], 0\n\t", "", "") \
  A_PERIOD(64, 80, KC, 120, 32, VC, 16, VC + 4, "", "", "", "s_waitcnt vmcnt(2)\n\t") \
  A_PERIOD(80, 64, KO, 96, 48, VC, 32, VC + 4, A_LD(KC, PC, 0) A_LD(KC + 4, PC, 1024), "", "", "s_add_u32 %[cnt], %[cnt], 1\n\t")

__global__ __launch_bounds__(320, 2) void k2_attn(const char* __restrict__ wsi, char* __restrict__ ws) {
  __shared__ __attribute__((aligned(1024))) char smem[K2_LDS];
  LDS_AS char* lds = (LDS_AS char*)smem;
  const int bid = blockIdx.x, xcd = bid & 7, jj = bid >> 3;
  const int head = 2 * xcd + (jj & 1), qb = jj >> 1;
  const int lane = threadIdx.x & 63, wave = __builtin_amdgcn_readfirstlane(threadIdx.x >> 6);
  const int h = lane >> 5;
  const int col = (jj >> 2) * 32 + xcd * 4 + (jj & 3);
  STAMP_DECL;
  if (wave == 4) {
    __builtin_amdgcn_s_sleep(A2_SLEEP);
    ST(0);
    if (col < DF) a2_column(col, lane, wsi, ws, lds + K2_TL);
    ST(1); STAMP_FLUSH(ws, 4);
    return;
  }
  ST(0);
  const int n0 = col < DF ? K2_N0 : 16, no = (64 - n0 + 2) / 3;
  const int st = wave == 0 ? 0 : n0 + (wave - 1) * no, nt = wave == 0 ? n0 : (wave == 3 ? 64 - st : no);
  const rsrc_t rkv = mkrsrc(wsi + WS_KV + head * 262144, 262144), rq = mkrsrc(wsi + WS_QF + (head * 64 + 4 * qb) * 2048, 8192);
  int offA = ((st + 4 * qb) & 63) << 12, offB = (offA + 4096) & 0x3ffff;
  const int l16 = lane * 16;
  u32x4 KA0 = bld(rkv, l16, offA), KA1 = bld(rkv, l16 + 1024, offA);
  u32x4 Q0[4], Q1[4];
#pragma unroll
  for (int t = 0; t < 4; ++t) { const int o = ((wave + t) & 3) * 2048; Q0[t] = bld(rq, l16, o); Q1[t] = bld(rq, l16 + 1024, o); }
  u32x4 VA0 = bld(rkv, l16 + 2048, offA), VA1 = bld(rkv, l16 + 3072, offA), KB0 = bld(rkv, l16, offB), KB1 = bld(rkv, l16 + 1024, offB);
  f32x16 O[4]; float mref[4];
#pragma unroll
  for (int t = 0; t < 4; ++t) {
    f32x16 s = MFMA(asb(KA0), asb(Q0[t]), zero16()); s = MFMA(asb(KA1), asb(Q1[t]), s);
    const float m = swapmax(max16(s));
    const unsigned nb = pk(-m, 0.f);
    mref[t] = -__uint_as_float(nb << 16);
    Q1[t][2] = h == 0 ? nb : Q1[t][2];
    O[t] = zero16();
  }
  ST(1);
  unsigned long long cy0 = CYC();
  u32x4 VB0, VB1 = {0, 0, 0, 0};
  {
    f32x16 SA = MFMA(asb(KA0), asb(Q0[0]), zero16()); SA = MFMA(asb(KA1), asb(Q1[0]), SA);
    int cnt = -nt;
    const uint64_t kvs = (uint64_t)(wsi + WS_KV + head * 262144);
    u32x4 P1 = {0, 0, 0, 0};
    asm volatile(
      "s_add_u32 s42, %[kvlo], %[off]\n\ts_addc_u32 s43, %[kvhi], 0\n\ts_nop 9\n"
      "Ltop%=:\n\t"
      A_HALF(128, 136, 144, 152, "s[40:41]", "s40", "s41", "s[42:43]")
      "s_cbranch_scc1 LendA%=\n\t"
      A_HALF(144, 152, 128, 136, "s[42:43]", "s42", "s43", "s[40:41]")
      "s_cbranch_scc0 Ltop%=\n\t"
      "s_nop 0\n\t"
      A_MF(48, 156, 164, A_ACC(48))
      "s_branch Ldone%=\n"
      "LendA%=:\n\t"
      "s_nop 0\n\t"
      A_MF(48, 140, 164, A_ACC(48))
      "Ldone%=:\n\t"
      "s_nop 11"
      : "+{v[0:15]}"(O[0]), "+{v[16:31]}"(O[1]), "+{v[32:47]}"(O[2]), "+{v[48:63]}"(O[3]), "+{v[64:79]}"(SA),
        "+{v[128:131]}"(KA0), "+{v[132:135]}"(KA1), "+{v[136:139]}"(VA0), "+{v[140:143]}"(VA1), "+{v[144:147]}"(KB0), "+{v[148:151]}"(KB1),
        "={v[152:155]}"(VB0), "+{v[156:159]}"(VB1), "+{v[164:167]}"(P1), [off] "+s"(offB), [cnt] "+s"(cnt)
      : "{v[96:99]}"(Q0[0]), "{v[100:103]}"(Q1[0]), "{v[104:107]}"(Q0[1]), "{v[108:111]}"(Q1[1]), "{v[112:115]}"(Q0[2]), "{v[116:119]}"(Q1[2]), "{v[120:123]}"(Q0[3]), "{v[124:127]}"(Q1[3]),
        "{v172}"(l16), [kvlo] "s"((unsigned)kvs), [kvhi] "s"((unsigned)(kvs >> 32))
      : "memory", "scc", "s40", "s41", "s42", "s43", "v80", "v81", "v82", "v83", "v84", "v85", "v86", "v87", "v88", "v89", "v90", "v91", "v92", "v93", "v94", "v95",
        "v160", "v161", "v162", "v163", "v168", "v169", "v170", "v171");
  }
  SC(5, cy0);
  ST(2);
#pragma unroll
  for (int t = 1; t < 4; ++t) {
    const int e = ((wave + t) & 3) * 3 + t - 1;
    LDS_AS char* p = lds + K2_EX + e * 4096 + l16;
#pragma unroll
    for (int g = 0; g < 4; ++g) *(LDS_AS f32x4*)(p + g * 1024) = (f32x4){O[t][4 * g], O[t][4 * g + 1], O[t][4 * g + 2], O[t][4 * g + 3]};
    *(LDS_AS float*)(lds + K2_MX + e * 256 + lane * 4) = mref[t];
  }
  asm volatile("s_waitcnt vmcnt(0) lgkmcnt(0)\n\ts_barrier"
               : "+{v[128:131]}"(KA0), "+{v[132:135]}"(KA1), "+{v[136:139]}"(VA0), "+{v[140:143]}"(VA1), "+{v[144:147]}"(KB0), "+{v[148:151]}"(KB1), "+{v[152:155]}"(VB0), "+{v[156:159]}"(VB1) :: "memory");
  ST(3);
  {
    f32x16 acc = O[0];
    float mw[3]; float M = mref[0];
#pragma unroll
    for (int k = 0; k < 3; ++k) { mw[k] = *(const LDS_AS float*)(lds + K2_MX + (wave * 3 + k) * 256 + lane * 4); M = fmaxf(M, mw[k]); }
    float lmin = acc[12], lmax = acc[12];
    const float fo = EX2(mref[0] - M);
#pragma unroll
    for (int i = 0; i < 16; ++i) acc[i] *= fo;
#pragma unroll
    for (int k = 0; k < 3; ++k) { const float f = EX2(mw[k] - M);
      const LDS_AS char* e = lds + K2_EX + (wave * 3 + k) * 4096 + l16;
#pragma unroll
      for (int g = 0; g < 4; ++g) { const f32x4 v = *(const LDS_AS f32x4*)(e + g * 1024);
        if (g == 3) { lmin = fminf(lmin, v[0]); lmax = fmaxf(lmax, v[0]); }
#pragma unroll
        for (int i = 0; i < 4; ++i) acc[4 * g + i] = fmaf(v[i], f, acc[4 * g + i]); } }
    float sm = 0.f;
#pragma unroll
    for (int i = 0; i < 16; ++i) sm += acc[i];
    const bool ok = (h == 1 || (lmin >= 0x1p-40f && lmax <= 0x1p40f)) && fabsf(sm) <= 3.0e38f;
    if (__builtin_expect(!__all(ok), 0)) {
      const bf16x8 q0 = asb(bld(rq, l16, wave * 2048)), q1 = asb(bld(rq, l16 + 1024, wave * 2048));
      float m = -INFINITY; acc = zero16();
      for (int kt = 0; kt < 64; ++kt) {
        f32x16 s = MFMA(asb(bld(rkv, l16, kt << 12)), q0, zero16()); s = MFMA(asb(bld(rkv, l16 + 1024, kt << 12)), q1, s);
        const float mn = fmaxf(m, swapmax(max16(s)));
        const float al = EX2(m - mn); m = mn;
#pragma unroll
        for (int i = 0; i < 16; ++i) { acc[i] *= al; s[i] = EX2(s[i] - mn); }
        acc = MFMA(asb(bld(rkv, l16 + 2048, kt << 12)), asb(pk8(s, 0)), acc); acc = MFMA(asb(bld(rkv, l16 + 3072, kt << 12)), asb(pk8(s, 1)), acc);
      }
    }
    auto rr = __builtin_amdgcn_permlane32_swap(__float_as_uint(acc[12]), __float_as_uint(acc[12]), false, false);
    const float inv = __builtin_amdgcn_rcpf(__uint_as_float(rr[0]));
#pragma unroll
    for (int i = 0; i < 16; ++i) acc[i] *= inv;
    u32x4 p0 = pk8(acc, 0), p1 = pk8(acc, 1);
    if (h == 0) { p1[2] = 0; p1[3] = 0; }
    char* dst = ws + WS_OF + ((4 * qb + wave) * 16 + head) * 2048 + l16;
    *(u32x4*)dst = p0; *(u32x4*)(dst + 1024) = p1;
  }
  ST(4);
  if (wave == 0) STAMP_FLUSH(ws, 1);
  if (wave == 1) STAMP_FLUSH(ws, 5);
}

__global__ __launch_bounds__(512, 2) void k3_out(const char* __restrict__ ws, const u16* __restrict__ wo, u16* __restrict__ out) {
  __shared__ __attribute__((aligned(1024))) char smem[57344];
  LDS_AS char* lds = (LDS_AS char*)smem;
  const int bid = blockIdx.x, xcd = bid & 7, jj = bid >> 3;
  const int part = xcd & 1, mt = 4 * (xcd >> 1) + (jj & 3), nt = jj >> 2;
  const int lane = threadIdx.x & 63, wave = __builtin_amdgcn_readfirstlane(threadIdx.x >> 6);
  if (wave >= 4) {
    const int lw = wave - 4, rr = lane >> 3, sl = lane & 7;
    const char* src; int pstride;
    if (part) { src = (const char*)wo + nt * 128 + rr * 896 + (((sl >> 2) ^ ((rr >> 1) & 1)) * 64) + (sl & 3) * 16 + lw * 8 * 896; pstride = 32 * 896; }
    else { src = nullptr; pstride = 0; }
#pragma unroll
    for (int i = 0; i < 14; ++i) {
      const int p = lw + 4 * i;
      const char* g;
      if (part) g = src + i * pstride;
      else { const int seg = p >> 3, row = 8 * (p & 7) + rr; g = (const char*)&g_F + (64 * nt + row) * 896 + seg * 128 + ((sl ^ ((row >> 1) & 7)) * 16); }
      dma16(g, lds + p * 1024);
    }
    asm volatile("s_waitcnt vmcnt(10)\n\ts_barrier" ::: "memory");
    asm volatile("s_waitcnt vmcnt(6)\n\ts_barrier" ::: "memory");
    asm volatile("s_waitcnt vmcnt(2)\n\ts_barrier" ::: "memory");
    asm volatile("s_waitcnt vmcnt(0)\n\ts_barrier" ::: "memory");
    return;
  }
  const int r = lane & 31, h = lane >> 5, tt = 4 * mt + wave;
  STAMP_DECL; ST(0);
  f32x16 acc0 = zero16(), acc1 = zero16();
  int tok, colb;
  if (part) {
    bf16x8 tk[32];
    const char* src = ws + WS_OF + tt * 32768 + lane * 16;
#pragma unroll
    for (int s = 0; s < 32; ++s) tk[s] = *(const bf16x8*)(src + s * 1024);
    const int q = (lane & 15) >> 2;
    const LDS_AS char* w0 = lds + (4 * h + q) * 128 + ((q >> 1) * 64) + ((lane >> 4) & 1) * 32 + (lane & 3) * 8;
    const LDS_AS char* w1 = lds + (4 * h + q) * 128 + (((q >> 1) ^ 1) * 64) + ((lane >> 4) & 1) * 32 + (lane & 3) * 8;
#pragma unroll
    for (int s = 0; s < 32; ++s) {
      if ((s & 7) == 0) asm volatile("s_barrier" ::: "memory");
      const int o = (s >> 1) * 3584 + (s & 1) * 2048, o2 = (s & 1) ? 512 : 1024;
      acc0 = MFMA(ldtr2(w0 + o, o2), tk[s], acc0); acc1 = MFMA(ldtr2(w1 + o, o2), tk[s], acc1);
    }
    tok = 32 * tt + r; colb = 448 + 64 * nt;
  } else {
    u32x4 tk[28];
    const char* src = ws + WS_Z2 + h * 4 * 8192 + (32 * tt + r) * 4;
#pragma unroll
    for (int s = 0; s < 28; ++s)
#pragma unroll
      for (int i = 0; i < 4; ++i) tk[s][i] = *(const unsigned*)(src + (8 * s + i) * 8192);
    const int sw = (r >> 1) & 7;
    const LDS_AS char* wa = lds + r * 128;
#pragma unroll
    for (int s = 0; s < 28; ++s) {
      if ((s & 7) == 0) asm volatile("s_barrier" ::: "memory");
      const LDS_AS char* p = wa + (s >> 2) * 8192 + (((2 * (s & 3) + h) ^ sw) * 16);
      acc0 = MFMA(ldsr(p), asb(tk[s]), acc0); acc1 = MFMA(ldsr(p + 4096), asb(tk[s]), acc1);
    }
    const int p = 32 * tt + r, l2 = (p >> 2) & 63, mg = p >> 8;
    tok = 64 * (l2 & 31) + 32 * (mg >> 2) + 8 * (mg & 3) + 4 * (l2 >> 5) + (p & 3); colb = 64 * nt;
  }
  ST(1);
  char* dst = (char*)out + tok * 1792 + colb * 2 + h * 16;
#pragma unroll
  for (int nb = 0; nb < 2; ++nb) {
    const f32x16& a = nb ? acc1 : acc0;
#pragma unroll
    for (int pr = 0; pr < 2; ++pr) {
      const unsigned a0 = pk(a[8 * pr], a[8 * pr + 1]), a1 = pk(a[8 * pr + 2], a[8 * pr + 3]), b0 = pk(a[8 * pr + 4], a[8 * pr + 5]), b1 = pk(a[8 * pr + 6], a[8 * pr + 7]);
      auto x = __builtin_amdgcn_permlane32_swap(a0, b0, false, false);
      auto y = __builtin_amdgcn_permlane32_swap(a1, b1, false, false);
      *(u32x4*)(dst + nb * 64 + pr * 32) = (u32x4){x[0], y[0], x[1], y[1]};
    }
  }
  ST(2);
  if (wave == 0) STAMP_FLUSH(ws, 2);
}

extern "C" void kernel_launch(void* const* d_in, const int* in_sizes, int n_in, void* d_out, int out_size, void* d_ws, size_t ws_size, hipStream_t stream) {
  char* ws = (char*)d_ws;
  k1_proj<<<256, 512, 0, stream>>>((const u16*)d_in[0], (const u16*)d_in[1], (const u16*)d_in[2], (const u16*)d_in[3], (const u16*)d_in[4], ws);
  k2_attn<<<256, 320, 0, stream>>>(ws, ws);
  k3_out<<<224, 512, 0, stream>>>(ws, (const u16*)d_in[5], (u16*)d_out);
}
```

```cpp
#include <hip/hip_runtime.h>
#include <stdint.h>

typedef unsigned short u16;
typedef short bf16x8 __attribute__((ext_vector_type(8)));
typedef short s16x4 __attribute__((ext_vector_type(4)));
typedef float f32x16 __attribute__((ext_vector_type(16)));
typedef float f32x4 __attribute__((ext_vector_type(4)));
typedef float f32x2 __attribute__((ext_vector_type(2)));
typedef unsigned u32x4 __attribute__((ext_vector_type(4)));
typedef unsigned u32x2 __attribute__((ext_vector_type(2)));
typedef __bf16 bf16x2_t __attribute__((ext_vector_type(2)));
#define LDS_AS __attribute__((address_space(3)))
#define DI __device__ __forceinline__
#define MFMA(a, b, c) __builtin_amdgcn_mfma_f32_32x32x16_bf16((a), (b), (c), 0, 0, 0)
#define SBAR() __builtin_amdgcn_sched_barrier(0)
#define PIN(x) asm volatile("" : "+v"(x))

constexpr int DF = 225;
#define K2_N0 13
#define K1_ROT 1
#define K1_PF 1
#define PRIO_M 0
#define PRIO_V 2
#define A2_SLEEP 8

namespace ct {
constexpr double PI = 3.14159265358979323846264338327950288;
constexpr double tsin(double x) { double x2 = x * x, t = x, s = x; for (int i = 1; i < 12; ++i) { t *= -x2 / ((2 * i) * (2 * i + 1)); s += t; } return s; }
constexpr double tcos(double x) { double x2 = x * x, t = 1, s = 1; for (int i = 1; i < 12; ++i) { t *= -x2 / ((2 * i - 1) * (2 * i)); s += t; } return s; }
constexpr double cos2pi(int j, int N) {
  j %= N; if (j < 0) j += N; if (2 * j > N) j = N - j; bool neg = false; if (4 * j > N) { j = N / 2 - j; neg = true; }
  double r = (8 * j > N) ? tsin(2 * PI * (N / 4 - j) / N) : tcos(2 * PI * j / N); return neg ? -r : r; }
constexpr double sin2pi(int j, int N) { return cos2pi(j - N / 4, N); }
constexpr u16 bf(double v) { float f = (float)v; unsigned u = __builtin_bit_cast(unsigned, f); u += 0x7fffu + ((u >> 16) & 1u); return (u16)(u >> 16); }
struct C448 { u16 c[448]; u16 ns[448]; };
constexpr C448 mk448() { C448 t{}; for (int j = 0; j < 448; ++j) { t.c[j] = bf(cos2pi(j, 448)); t.ns[j] = bf(-sin2pi(j, 448)); } return t; }
constexpr C448 T448 = mk448();
struct FPart { u16 v[56 * 448]; };
constexpr FPart mkF(int part) { FPart t{};
  for (int r = 0; r < 56; ++r) { int d = part * 56 + r; int a = 0; u16* o = &t.v[r * 448];
    for (int c = 0; c < 224; ++c) { o[2 * c] = T448.c[a]; o[2 * c + 1] = T448.ns[a]; a += d; if (a >= 448) a -= 448; }
    o[1] = (d & 1) ? 0xbf80 : 0x3f80; }
  return t; }
constexpr FPart F0 = mkF(0), F1 = mkF(1), F2 = mkF(2), F3 = mkF(3), F4 = mkF(4), F5 = mkF(5), F6 = mkF(6), F7 = mkF(7);
struct FTab { FPart p0, p1, p2, p3, p4, p5, p6, p7; u16 pad[128]; };
constexpr int kperm(int t, int h, int j) { return 16 * t + 8 * (j >> 2) + 4 * h + (j & 3); }
struct A2Tab {
  u16 t32f[3][2][64][8];
  u16 t64[3][2][4][64][8];
  u16 t32i[3][2][64][8];
};
constexpr A2Tab mkA2() { A2Tab t{};
  const double SC = 2.0 / (2048.0 * 448.0);
  for (int s = 0; s < 2; ++s) for (int l = 0; l < 64; ++l) for (int j = 0; j < 8; ++j) {
    int a = kperm(s, l >> 5, j) * (l & 31);
    t.t32f[0][s][l][j] = bf(cos2pi(a, 32)); t.t32f[1][s][l][j] = bf(sin2pi(a, 32)); t.t32f[2][s][l][j] = bf(-sin2pi(a, 32));
    int b = (16 * s + 8 * (l >> 5) + j) * (l & 31);
    t.t32i[0][s][l][j] = bf(SC * cos2pi(b, 32)); t.t32i[1][s][l][j] = bf(SC * sin2pi(b, 32)); t.t32i[2][s][l][j] = bf(-SC * sin2pi(b, 32)); }
  for (int m = 0; m < 2; ++m) for (int k = 0; k < 4; ++k) for (int l = 0; l < 64; ++l) for (int j = 0; j < 8; ++j) {
    int a = (32 * m + (l & 31)) * (32 * (k >> 1) + kperm(k & 1, l >> 5, j));
    t.t64[0][m][k][l][j] = bf(cos2pi(a, 64)); t.t64[1][m][k][l][j] = bf(sin2pi(a, 64)); t.t64[2][m][k][l][j] = bf(-sin2pi(a, 64)); }
  return t; }
struct TwTab { float tw[2][4][2][64][4]; };
constexpr TwTab mkTw() { TwTab t{};
  for (int m = 0; m < 2; ++m) for (int g = 0; g < 4; ++g) for (int l = 0; l < 64; ++l) for (int i = 0; i < 4; ++i) {
    int a = (32 * m + 8 * g + 4 * (l >> 5) + i) * (l & 31);
    t.tw[m][g][0][l][i] = (float)cos2pi(a, 2048); t.tw[m][g][1][l][i] = (float)sin2pi(a, 2048); }
  return t; }
}
__device__ __attribute__((aligned(256))) const ct::FTab g_F = { ct::F0, ct::F1, ct::F2, ct::F3, ct::F4, ct::F5, ct::F6, ct::F7, {} };
__device__ __attribute__((aligned(256))) const ct::A2Tab g_A2 = ct::mkA2();
__device__ __attribute__((aligned(256))) const ct::TwTab g_TW = ct::mkTw();

constexpr size_t WS_QF = 0;
constexpr size_t WS_KV = WS_QF + (2u << 20);
constexpr size_t WS_X1 = WS_KV + (4u << 20);
constexpr size_t WS_WT = WS_X1 + (2u << 20);
constexpr size_t WS_OF = WS_WT + (2u << 20);
constexpr size_t WS_Z2 = WS_OF + (2u << 20);

#define STAMP_DECL
#define ST(s) do {} while (0)
#define SC(s, base) do {} while (0)
#define CYC() 0ull
#define STAMP_FLUSH(wsp, k) do {} while (0)
DI unsigned pk(float a, float b) { f32x2 v = {a, b}; return __builtin_bit_cast(unsigned, __builtin_convertvector(v, bf16x2_t)); }
DI u32x4 pk8(const f32x16& x, int s) { u32x4 r; r[0] = pk(x[8 * s], x[8 * s + 1]); r[1] = pk(x[8 * s + 2], x[8 * s + 3]); r[2] = pk(x[8 * s + 4], x[8 * s + 5]); r[3] = pk(x[8 * s + 6], x[8 * s + 7]); return r; }
DI bf16x8 asb(u32x4 v) { return __builtin_bit_cast(bf16x8, v); }
DI bf16x8 ldsr(const LDS_AS char* p) { return *(const LDS_AS bf16x8*)p; }
DI s16x4 ldtr(const LDS_AS char* p) { return __builtin_amdgcn_ds_read_tr16_b64_v4i16((LDS_AS s16x4*)p); }
DI bf16x8 ldtr2(const LDS_AS char* p, int off2) { s16x4 lo = ldtr(p), hi = ldtr(p + off2); return __builtin_shufflevector(lo, hi, 0, 1, 2, 3, 4, 5, 6, 7); }
DI void dma16(const char* g, LDS_AS char* l) { __builtin_amdgcn_global_load_lds((const __attribute__((address_space(1))) unsigned*)g, (LDS_AS unsigned*)l, 16, 0, 0); }
#define WAITV(n) asm volatile("s_waitcnt vmcnt(" #n ")" ::: "memory")
#define BARRIER() asm volatile("s_waitcnt lgkmcnt(0)\n\ts_barrier" ::: "memory")
DI const f32x16 zero16() { f32x16 z; for (int i = 0; i < 16; ++i) z[i] = 0.f; return z; }

#define ST_POL "sc1"
#define OUT_POL ""
DI void st16(void* p, u32x4 v) { asm volatile("global_store_dwordx4 %0, %1, off " ST_POL "\n\ts_nop 1" :: "v"(p), "v"(v) : "memory"); }
DI void st16o(void* p, u32x4 v) { asm volatile("global_store_dwordx4 %0, %1, off " OUT_POL "\n\ts_nop 1" :: "v"(p), "v"(v) : "memory"); }
typedef __amdgpu_buffer_rsrc_t rsrc_t;
DI rsrc_t mkrsrc(const void* p, unsigned bytes) { return __builtin_amdgcn_make_buffer_rsrc((void*)p, 0, bytes, 0x00020000); }
DI u32x4 bld(rsrc_t r, int voff, int soff) { return __builtin_amdgcn_raw_buffer_load_b128(r, voff, soff, 0); }

constexpr int K1_SLOT = 30720;
template <bool TRANS, int WROW>
DI void k1_loop(const LDS_AS char* lds, int lane, int wm, int wn, f32x16 (&acc)[2][2], unsigned long long* sto) {
  constexpr int SLOT = 16384 + 64 * WROW, BLK = WROW / 4;
  const int r = lane & 31, h = lane >> 5;
  const int sw = (r >> 1) & 7;
  const LDS_AS char* xa = lds + (wm * 64 + r) * 128;
  int xo[4];
#pragma unroll
  for (int s = 0; s < 4; ++s) xo[s] = ((2 * s + h) ^ sw) * 16;
  const int q = (lane & 15) >> 2, c4 = ((lane >> 4) & 1) * 4 + (lane & 3);
  const LDS_AS char *wb0, *wb1; int t1;
  if (WROW == 224) {
    wb0 = lds + 16384 + (8 * h + 2 * q) * 224 + 2 * wn * 56 + (c4 >= 6 ? c4 - 1 : c4) * 8; wb1 = wb0 + 56; t1 = 224;
  } else {
    wb0 = lds + 16384 + (8 * h + q) * 256 + (((2 * wn) ^ q) * 64) + c4 * 8; wb1 = lds + 16384 + (8 * h + q) * 256 + (((2 * wn + 1) ^ q) * 64) + c4 * 8; t1 = 1024;
  }
  bf16x8 a0, a1, b0, b1, na0, na1, nb0, nb1;
  PIN(acc[0][0]); PIN(acc[0][1]); PIN(acc[1][0]); PIN(acc[1][1]);
  STAMP_DECL; unsigned long long c0 = CYC();
  BARRIER();
  SC(0, c0);
  a0 = ldsr(xa + xo[0]); b0 = ldtr2(wb0, t1); b1 = ldtr2(wb1, t1); a1 = ldsr(xa + xo[0] + 4096);
  int so = 0;
#define K1_MF(i, j, A, B) acc[i][j] = TRANS ? MFMA(B, A, acc[i][j]) : MFMA(A, B, acc[i][j])
#pragma unroll 1
  for (int c = 0; c < 7; ++c) {
#pragma unroll
    for (int s = 0; s < 4; ++s) {
      int xoff, woff;
      if (s < 3) { xoff = so + xo[s + 1]; woff = so + (s + 1) * 16 * WROW; }
      else {
        if (c < 6) BARRIER();
        SC(c + 1, c0);
        so = (so == 3 * SLOT) ? 0 : so + SLOT;
        xoff = so + xo[0]; woff = so;
      }
      SBAR();
      K1_MF(0, 0, a0, b0); SBAR();
      na0 = ldsr(xa + xoff); nb0 = ldtr2(wb0 + woff, t1); SBAR();
      K1_MF(0, 1, a0, b1); SBAR();
      nb1 = ldtr2(wb1 + woff, t1); na1 = ldsr(xa + xoff + 4096); SBAR();
      K1_MF(1, 0, a1, b0); SBAR();
      K1_MF(1, 1, a1, b1); SBAR();
      a0 = na0; a1 = na1; b0 = nb0; b1 = nb1;
    }
  }
}

template <int NW>
DI void k1_loader(LDS_AS char* lds, int lane, int lw, int bid, int mt, int nt, const u16* ax, const u16* wr, const u16* wi, const u16* bx, const u16* wqkv, char* ws) {
  const bool spec = nt >= 12;
  const char* xb = (const char*)(spec ? ax : bx);
  const char* wbase = spec ? (const char*)&g_F + (nt - 12) * 256 : (const char*)wqkv + nt * 224;
  const int ldb = spec ? 896 : 2688, slot = spec ? 32768 : 30720;
  unsigned vx[4], vw[4];
  int wp0;
#pragma unroll
  for (int i = 0; i < 4; ++i) {
    const int row = 8 * (lw * 4 + i) + (lane >> 3), kg = (lane & 7) ^ ((row >> 1) & 7);
    const int tok = spec ? 64 * (row & 31) + 4 * mt + (row >> 5) : 128 * mt + row;
    vx[i] = tok * 896 + kg * 16;
  }
  if (spec) {
    wp0 = lw * 4;
#pragma unroll
    for (int i = 0; i < 4; ++i) { const int wrow = 4 * (wp0 + i) + (lane >> 4); vw[i] = wrow * 896 + (((lane >> 2) & 3) ^ (wrow & 3)) * 64 + (lane & 3) * 16; }
  } else {
    wp0 = lw < 2 ? lw * 4 : 8 + (lw - 2) * 3;
#pragma unroll
    for (int i = 0; i < NW; ++i) { const int idx = 64 * (wp0 + i) + lane, pos = (idx * 4682) >> 16, pc = idx - 14 * pos;
      const int k = (pos & ~7) | ((pos & 1) << 2) | ((pos >> 1) & 3); vw[i] = k * 2688 + pc * 16; }
  }
  const int rot = K1_ROT ? (2 * (nt & 3) + (mt & 7)) % 7 : 0;
  auto issue = [&](int c) {
    const int cs = c + rot >= 7 ? c + rot - 7 : c + rot;
    const char* xs = xb + cs * 128; const char* wsrc = wbase + cs * 64 * ldb;
    LDS_AS char* l = lds + (c & 3) * slot;
#pragma unroll
    for (int i = 0; i < 4; ++i) dma16(xs + vx[i], l + (lw * 4 + i) * 1024);
#pragma unroll
    for (int i = 0; i < NW; ++i) dma16(wsrc + vw[i], l + 16384 + (wp0 + i) * 1024);
  };
  issue(0); issue(1); issue(2);
  unsigned wv[2][8]; int wc[2], wf1[2];
  const int a = bid >> 4;
  { const int o = (bid >> 2) & 3, cg = bid & 3;
#pragma unroll
    for (int k = 0; k < 2; ++k) {
      int u = lw * 64 + lane + 256 * k; u = u > 455 ? 455 : u;
      const int f1l = (u * 1150) >> 16, cl = u - 57 * f1l;
      int c = 57 * cg + cl; c = c > 224 ? 224 : c;
      wc[k] = c; wf1[k] = 8 * o + f1l;
      const int off = ((128 * a + 8 * o + f1l) * DF + c) * 2;
#pragma unroll
      for (int i = 0; i < 4; ++i) {
        asm volatile("global_load_ushort %0, %1, %2" : "=v"(wv[k][2 * i]) : "v"(off + i * 32 * DF * 2), "s"(wr));
        asm volatile("global_load_ushort %0, %1, %2" : "=v"(wv[k][2 * i + 1]) : "v"(off + i * 32 * DF * 2), "s"(wi));
      }
    } }
  constexpr int P = 4 + NW;
#define K1_WB(n) asm volatile("s_waitcnt vmcnt(%0)\n\ts_barrier" :: "i"(n) : "memory")
  K1_WB(2 * P + 16);
  issue(3);
  K1_WB(2 * P + 16);
  issue(4);
  K1_WB(2 * P + 16);
  issue(5);
  asm volatile("s_waitcnt vmcnt(%16)\n\ts_barrier"
               : "+v"(wv[0][0]), "+v"(wv[0][1]), "+v"(wv[0][2]), "+v"(wv[0][3]), "+v"(wv[0][4]), "+v"(wv[0][5]), "+v"(wv[0][6]), "+v"(wv[0][7]),
                 "+v"(wv[1][0]), "+v"(wv[1][1]), "+v"(wv[1][2]), "+v"(wv[1][3]), "+v"(wv[1][4]), "+v"(wv[1][5]), "+v"(wv[1][6]), "+v"(wv[1][7]) : "i"(2 * P) : "memory");
  issue(6);
  K1_WB(2 * P);
  K1_WB(P);
  K1_WB(0);
#pragma unroll
  for (int k = 0; k < 2; ++k) {
    u32x4 o;
    const bool half = (wc[k] == 0) || (wc[k] == 224);
#pragma unroll
    for (int i = 0; i < 4; ++i) {
      unsigned re = wv[k][2 * i] << 16, im = wv[k][2 * i + 1] << 16;
      if (half) { re = __float_as_uint(__uint_as_float(re) * 0.5f); im = __float_as_uint(__uint_as_float(im) * 0.5f); }
      o[i] = (re >> 16) | (im & 0xffff0000u);
    }
    st16(ws + WS_WT + wc[k] * 8192 + ((a >> 1) * 64 + wf1[k] + 32 * (a & 1)) * 16, o);
  }
}

__global__ __launch_bounds__(512, 2) void k1_proj(const u16* __restrict__ ax, const u16* __restrict__ wr, const u16* __restrict__ wi,
                                                  const u16* __restrict__ bx, const u16* __restrict__ wqkv, char* __restrict__ ws) {
  __shared__ __attribute__((aligned(1024))) char smem[4 * 32768];
  LDS_AS char* lds = (LDS_AS char*)smem;
  const int bid = blockIdx.x, xcd = bid & 7, jj = bid >> 3;
  const int mt = 8 * (xcd & 1) + (jj & 7), nt = 4 * (xcd >> 1) + (jj >> 3), type = nt >> 2;
  const int lane = threadIdx.x & 63, wave = __builtin_amdgcn_readfirstlane(threadIdx.x >> 6);
  if (wave >= 4) {
    if (type == 3 || wave < 6) k1_loader<4>(lds, lane, wave - 4, bid, mt, nt, ax, wr, wi, bx, wqkv, ws);
    else k1_loader<3>(lds, lane, wave - 4, bid, mt, nt, ax, wr, wi, bx, wqkv, ws);
    return;
  }
  const int wm = wave & 1, wn = wave >> 1, r = lane & 31, h = lane >> 5;
  STAMP_DECL; unsigned long long sto[8]; ST(0);
  unsigned pf0 = 0, pf1 = 0;
  if (K1_PF) {
    int i = wave * 64 + lane; i = i > 223 ? 223 : i;
    const int L = (nt & 3) * 224 + i, row = (L * 9363) >> 16, seg = L - 7 * row;
    const int tok = type == 3 ? 64 * (row & 31) + 4 * mt + (row >> 5) : 128 * mt + row;
    asm volatile("global_load_dword %0, %1, %2" : "=v"(pf0) : "v"(tok * 896 + seg * 128), "s"(type == 3 ? ax : bx));
    int j = wave * 64 + lane; j = j > 167 ? 167 : j;
    const int wrow = (j * 21846) >> 16, ls = j - 3 * wrow;
    const int woff = (56 * (mt & 7) + wrow) * (type == 3 ? 896 : 2688) + (ls == 2 ? 220 : ls * 128);
    asm volatile("global_load_dword %0, %1, %2" : "=v"(pf1) : "v"(woff), "s"(type == 3 ? (const char*)&g_F + (nt - 12) * 256 : (const char*)wqkv + nt * 224));
  }
  f32x16 acc[2][2];
#pragma unroll
  for (int i = 0; i < 2; ++i)
#pragma unroll
    for (int j = 0; j < 2; ++j) acc[i][j] = zero16();
  if (type < 2) k1_loop<true, 224>(lds, lane, wm, wn, acc, sto); else if (type == 2) k1_loop<false, 224>(lds, lane, wm, wn, acc, sto); else k1_loop<false, 256>(lds, lane, wm, wn, acc, sto);
  asm volatile("s_waitcnt vmcnt(0)" : "+v"(pf0), "+v"(pf1));
  ST(1);
  const float qs = type == 0 ? 0.18898223650461362f * 1.4426950408889634f : 1.0f;
  const bool padqk = type < 2 && h == 0, padv = type == 2 && (r >> 2) == 6;
  const unsigned kone = type == 1 ? 0x3f80u : 0u, vone = r == 24 ? 0x3f803f80u : 0u;
  char* dst0 = type == 0 ? ws + WS_QF + (4 * nt * 64) * 2048 : type == 3 ? ws + WS_X1 + (4 * (nt - 12) * 64) * 2048 : ws + WS_KV + (4 * (nt & 3) * 64) * 4096 + (type == 2 ? 2048 : 0);
  const int tsz = (type == 0 || type == 3) ? 2048 : 4096;
  dst0 += (2 * wn * 64 + 4 * mt + 2 * wm) * tsz + lane * 16;
  if (nt == 15 && wn == 1) return;
#pragma unroll
  for (int mi = 0; mi < 2; ++mi)
#pragma unroll
    for (int ni = 0; ni < 2; ++ni) {
      f32x16 v = acc[mi][ni];
#pragma unroll
      for (int i = 0; i < 16; ++i) v[i] *= qs;
      u32x4 p0 = pk8(v, 0), p1 = pk8(v, 1);
      if (padqk) { p1[2] = kone; p1[3] = 0; }
      if (padv) { p0 = (u32x4){vone, vone, vone, vone}; p1 = p0; }
      char* dst = dst0 + (ni * 64 + mi) * tsz;
      st16(dst, p0); st16(dst + 1024, p1);
    }
  ST(2);
}

DI float swapmax(float m) { auto rr = __builtin_amdgcn_permlane32_swap(__float_as_uint(m), __float_as_uint(m), false, false); return fmaxf(__uint_as_float(rr[0]), __uint_as_float(rr[1])); }
DI float max16(const f32x16& s) {
  float a = fmaxf(fmaxf(s[0], s[1]), s[2]), b = fmaxf(fmaxf(s[3], s[4]), s[5]);
  a = fmaxf(fmaxf(a, s[6]), s[7]); b = fmaxf(fmaxf(b, s[8]), s[9]); a = fmaxf(fmaxf(a, s[10]), s[11]); b = fmaxf(fmaxf(b, s[12]), s[13]);
  a = fmaxf(fmaxf(a, s[14]), s[15]); return fmaxf(a, b); }
#define EX2(x) __builtin_amdgcn_exp2f(x)

template <bool CONJ>
DI void cmul2(float a0, float b0, float c0, float s0, float a1, float b1, float c1, float s1, unsigned& re, unsigned& im) {
  float t0, t1, t2, t3;
  if (CONJ)
    asm("v_mul_f32 %2, %7, %9\n\tv_mul_f32 %3, %6, %9\n\tv_mul_f32 %4, %11, %13\n\tv_mul_f32 %5, %10, %13\n\t"
        "v_fma_f32 %2, %6, %8, %2\n\tv_fma_f32 %3, %7, %8, -%3\n\tv_fma_f32 %4, %10, %12, %4\n\tv_fma_f32 %5, %11, %12, -%5\n\t"
        "v_cvt_pk_bf16_f32 %0, %2, %4\n\tv_cvt_pk_bf16_f32 %1, %3, %5"
        : "=v"(re), "=v"(im), "=&v"(t0), "=&v"(t1), "=&v"(t2), "=&v"(t3) : "v"(a0), "v"(b0), "v"(c0), "v"(s0), "v"(a1), "v"(b1), "v"(c1), "v"(s1));
  else
    asm("v_mul_f32 %2, %7, %9\n\tv_mul_f32 %3, %6, %9\n\tv_mul_f32 %4, %11, %13\n\tv_mul_f32 %5, %10, %13\n\t"
        "v_fma_f32 %2, %6, %8, -%2\n\tv_fma_f32 %3, %7, %8, %3\n\tv_fma_f32 %4, %10, %12, -%4\n\tv_fma_f32 %5, %11, %12, %5\n\t"
        "v_cvt_pk_bf16_f32 %0, %2, %4\n\tv_cvt_pk_bf16_f32 %1, %3, %5"
        : "=v"(re), "=v"(im), "=&v"(t0), "=&v"(t1), "=&v"(t2), "=&v"(t3) : "v"(a0), "v"(b0), "v"(c0), "v"(s0), "v"(a1), "v"(b1), "v"(c1), "v"(s1));
}
#define MF_DONE(x, y, z, w) do { __builtin_amdgcn_s_setprio(PRIO_V); asm volatile("s_nop 11" : "+v"(x), "+v"(y), "+v"(z), "+v"(w)); SBAR(); } while (0)
#define VA_DONE(b) do { __builtin_amdgcn_s_setprio(PRIO_M); asm volatile("s_nop 1" : "+v"(b[0]), "+v"(b[1]), "+v"(b[2]), "+v"(b[3]), "+v"(b[4]), "+v"(b[5]), "+v"(b[6]), "+v"(b[7])); SBAR(); } while (0)

DI void a2_column(int c, int lane, const char* __restrict__ ws_in, char* __restrict__ ws, LDS_AS char* tl) {
  const int r = lane & 31, h = lane >> 5, l16 = lane * 16;
  const bool real_col = (c == 0) || (c == 224);
  const int cp = c == 224 ? 1 : 2 * c;
  const rsrc_t rx = mkrsrc(ws_in + WS_X1 + (cp >> 5) * 131072 + (cp & 31) * 16, 0x40000u);
  const rsrc_t rt = mkrsrc(&g_A2, sizeof(g_A2)), rw = mkrsrc(&g_TW, sizeof(g_TW)), rf = mkrsrc(ws_in + WS_WT + c * 8192, 8192);
  const int xv = r * 2048 + h * 512;
#define T32F(mat, t) asb(bld(rt, l16, ((mat) * 2 + (t)) * 1024))
#define T64(mat, m, k) asb(bld(rt, l16, 6144 + (((mat) * 2 + (m)) * 4 + (k)) * 1024))
#define T32I(mat, t) asb(bld(rt, l16, 6144 + 24576 + ((mat) * 2 + (t)) * 1024))
#define TW(m, g, p) __builtin_bit_cast(f32x4, bld(rw, l16, (((m) * 4 + (g)) * 2 + (p)) * 1024))
  bf16x8 ur[2][2], ui[2][2], t32[3][2], t64[3][2][4]; f32x4 tw[2][4][2]; u32x4 wv[2][4];
#pragma unroll
  for (int m = 0; m < 2; ++m)
#pragma unroll
    for (int t = 0; t < 2; ++t) { ur[m][t] = asb(bld(rx, xv, m * 65536 + t * 1024)); ui[m][t] = asb(bld(rx, xv, m * 65536 + t * 1024 + 16)); }
#pragma unroll
  for (int i = 0; i < 6; ++i) t32[i >> 1][i & 1] = T32F(i >> 1, i & 1);
#pragma unroll
  for (int i = 0; i < 16; ++i) tw[i >> 3][(i >> 1) & 3][i & 1] = TW(i >> 3, (i >> 1) & 3, i & 1);
  SBAR();
  if (real_col) {
#pragma unroll
    for (int m = 0; m < 2; ++m)
#pragma unroll
      for (int t = 0; t < 2; ++t) ui[m][t] = (bf16x8){0, 0, 0, 0, 0, 0, 0, 0};
  }
  __builtin_amdgcn_s_setprio(PRIO_M);
  f32x16 re[2], im[2];
#pragma unroll
  for (int m = 0; m < 2; ++m) {
    re[m] = MFMA(ur[m][0], t32[0][0], zero16()); im[m] = MFMA(ui[m][0], t32[0][0], zero16());
    re[m] = MFMA(ur[m][1], t32[0][1], re[m]);    im[m] = MFMA(ui[m][1], t32[0][1], im[m]);
    re[m] = MFMA(ui[m][0], t32[1][0], re[m]);    im[m] = MFMA(ur[m][0], t32[2][0], im[m]);
    re[m] = MFMA(ui[m][1], t32[1][1], re[m]);    im[m] = MFMA(ur[m][1], t32[2][1], im[m]);
  }
  SBAR();
#pragma unroll
  for (int i = 0; i < 12; ++i) t64[i >> 2][0][i & 3] = T64(i >> 2, 0, i & 3);
  MF_DONE(re[0], im[0], re[1], im[1]);
  u32x4 b[8];
#pragma unroll
  for (int m = 0; m < 2; ++m)
#pragma unroll
    for (int e = 0; e < 16; e += 2) { const f32x4 cs = tw[m][e >> 2][0], sn = tw[m][e >> 2][1]; unsigned pr, pi;
      cmul2<true>(re[m][e], im[m][e], cs[e & 3], sn[e & 3], re[m][e + 1], im[m][e + 1], cs[(e & 3) + 1], sn[(e & 3) + 1], pr, pi);
      b[2 * m + (e >> 3)][(e >> 1) & 3] = pr; b[4 + 2 * m + (e >> 3)][(e >> 1) & 3] = pi; }
  SBAR();
#pragma unroll
  for (int i = 0; i < 12; ++i) t64[i >> 2][1][i & 3] = T64(i >> 2, 1, i & 3);
#pragma unroll
  for (int i = 0; i < 8; ++i) wv[i >> 2][i & 3] = bld(rf, l16, i * 1024);
  VA_DONE(b);
#pragma unroll
  for (int m = 0; m < 2; ++m) {
    re[m] = zero16(); im[m] = zero16();
#pragma unroll
    for (int k = 0; k < 4; ++k) {
      re[m] = MFMA(t64[0][m][k], asb(b[k]), re[m]); im[m] = MFMA(t64[0][m][k], asb(b[4 + k]), im[m]);
      re[m] = MFMA(t64[1][m][k], asb(b[4 + k]), re[m]); im[m] = MFMA(t64[2][m][k], asb(b[k]), im[m]);
    }
  }
  SBAR();
  { int l2 = l16; asm volatile("" : "+v"(l2));
#pragma unroll
    for (int i = 0; i < 12; ++i) t64[i >> 2][0][i & 3] = asb(bld(rt, l2, 6144 + ((i >> 2) * 8 + (i & 3)) * 1024)); }
  MF_DONE(re[0], im[0], re[1], im[1]);
  int l3 = l16; asm volatile("" : "+v"(l3));
#pragma unroll
  for (int m = 0; m < 2; ++m)
#pragma unroll
    for (int e = 0; e < 16; e += 2) { const unsigned w0 = wv[m][e >> 2][e & 3], w1 = wv[m][e >> 2][(e & 3) + 1]; unsigned pr, pi;
      cmul2<false>(re[m][e], im[m][e], __uint_as_float(w0 << 16), __uint_as_float(w0 & 0xffff0000u), re[m][e + 1], im[m][e + 1], __uint_as_float(w1 << 16), __uint_as_float(w1 & 0xffff0000u), pr, pi);
      b[2 * m + (e >> 3)][(e >> 1) & 3] = pr; b[4 + 2 * m + (e >> 3)][(e >> 1) & 3] = pi; }
  SBAR();
#pragma unroll
  for (int i = 0; i < 8; ++i) tw[0][i >> 1][i & 1] = __builtin_bit_cast(f32x4, bld(rw, l3, i * 1024));
  VA_DONE(b);
#pragma unroll
  for (int m = 0; m < 2; ++m) {
    re[m] = zero16(); im[m] = zero16();
#pragma unroll
    for (int k = 0; k < 4; ++k) {
      re[m] = MFMA(t64[0][m][k], asb(b[k]), re[m]); im[m] = MFMA(t64[0][m][k], asb(b[4 + k]), im[m]);
      re[m] = MFMA(t64[2][m][k], asb(b[4 + k]), re[m]); im[m] = MFMA(t64[1][m][k], asb(b[k]), im[m]);
    }
    SBAR();
    if (m == 0) {
#pragma unroll
      for (int i = 0; i < 8; ++i) tw[1][i >> 1][i & 1] = __builtin_bit_cast(f32x4, bld(rw, l3, (8 + i) * 1024));
      SBAR();
    }
  }
#pragma unroll
  for (int i = 0; i < 6; ++i) t32[i >> 1][i & 1] = T32I(i >> 1, i & 1);
  MF_DONE(re[0], im[0], re[1], im[1]);
#pragma unroll
  for (int m = 0; m < 2; ++m)
#pragma unroll
    for (int g = 0; g < 4; ++g) { const f32x4 cs = tw[m][g][0], sn = tw[m][g][1]; u32x2 pr, pi; unsigned x, y;
      cmul2<false>(re[m][4 * g], im[m][4 * g], cs[0], sn[0], re[m][4 * g + 1], im[m][4 * g + 1], cs[1], sn[1], x, y); pr[0] = x; pi[0] = y;
      cmul2<false>(re[m][4 * g + 2], im[m][4 * g + 2], cs[2], sn[2], re[m][4 * g + 3], im[m][4 * g + 3], cs[3], sn[3], x, y); pr[1] = x; pi[1] = y;
      LDS_AS char* p = tl + r * 192 + (32 * m + 8 * g + 4 * h) * 2;
      *(LDS_AS u32x2*)p = pr; *(LDS_AS u32x2*)(p + 6144) = pi;
    }
  __builtin_amdgcn_s_setprio(PRIO_M);
  asm volatile("s_waitcnt lgkmcnt(0)" ::: "memory");
  const LDS_AS char* tp = tl + (8 * h + ((lane & 15) >> 2)) * 192 + ((lane >> 4) & 1) * 32 + (lane & 3) * 8;
#pragma unroll
  for (int m = 0; m < 2; ++m) {
    re[m] = zero16(); im[m] = zero16();
#pragma unroll
    for (int t = 0; t < 2; ++t) {
      const bf16x8 ar = ldtr2(tp + t * 16 * 192 + m * 64, 4 * 192), ai = ldtr2(tp + 6144 + t * 16 * 192 + m * 64, 4 * 192);
      re[m] = MFMA(ar, t32[0][t], re[m]); im[m] = MFMA(ai, t32[0][t], im[m]);
      re[m] = MFMA(ai, t32[2][t], re[m]); im[m] = MFMA(ar, t32[1][t], im[m]);
    }
  }
  SBAR(); __builtin_amdgcn_s_setprio(PRIO_V);
  if (!real_col) {
    char* z = ws + WS_Z2 + c * 8192 + lane * 16;
#pragma unroll
    for (int m = 0; m < 2; ++m)
#pragma unroll
      for (int g = 0; g < 4; ++g)
        st16(z + (m * 4 + g) * 1024, (u32x4){pk(re[m][4 * g], im[m][4 * g]), pk(re[m][4 * g + 1], im[m][4 * g + 1]), pk(re[m][4 * g + 2], im[m][4 * g + 2]), pk(re[m][4 * g + 3], im[m][4 * g + 3])});
  } else {
    char* z = ws + WS_Z2 + lane * 16 + (c ? 2 : 0);
#pragma unroll
    for (int m = 0; m < 2; ++m)
#pragma unroll
      for (int g = 0; g < 16; ++g) *(u16*)(z + (m * 4 + (g >> 2)) * 1024 + (g & 3) * 4) = (u16)pk(re[m][g], 0.f);
  }
}

constexpr int K2_EX = 0, K2_MX = 49152, K2_TL = K2_MX + 3072, K2_LDS = K2_TL + 12288;

#define A_MF(d, a, b, c) "v_mfma_f32_32x32x16_bf16 v[" #d ":" #d "+15], v[" #a ":" #a "+3], v[" #b ":" #b "+3], " c "\n\t"
#define A_ACC(d) "v[" #d ":" #d "+15]"
#define A_EXP(s, p, X) X "v_exp_f32 v168, v[" #s "]\n\tv_exp_f32 v169, v[" #s "+1]\n\tv_exp_f32 v170, v[" #s "+2]\n\tv_exp_f32 v171, v[" #s "+3]\n\t" \
                    "v_cvt_pk_bf16_f32 v[" #p "], v168, v169\n\tv_cvt_pk_bf16_f32 v[" #p "+1], v170, v171\n\t"
#define A_PERIOD(SC, SN, KN, QN, OC, V0C, OP, V1P, XA, XB, XC, XD) \
  A_MF(SN, KN, QN, "0")                 A_EXP(SC, 160, XA) \
  A_MF(OP, V1P, 164, A_ACC(OP))         A_EXP(SC + 4, 162, XB) \
  A_MF(SN, KN + 4, QN + 4, A_ACC(SN))   A_EXP(SC + 8, 164, XC) \
  A_MF(OC, V0C, 160, A_ACC(OC))         A_EXP(SC + 12, 166, XD)
#define A_LD(d, so, off) "global_load_dwordx4 v[" #d ":" #d "+3], v172, " so " offset:" #off "\n\t"
#define A_HALF(KC, VC, KO, VO, PC, PC0, PC1, PO)   \
  A_PERIOD(64, 80, KC, 104, 0, VC, 48, VO + 4, "", A_LD(VO, PO, 2048) A_LD(VO + 4, PO, 3072), "s_waitcnt vmcnt(4)\n\t", "") \
  A_PERIOD(80, 64, KC, 112, 16, VC, 0, VC + 4, "s_add_u32 %[off], %[off], 0x1000\n\ts_and_b32 %[off], %[off], 0x3ffff\n\t", "s_add_u32 " PC0 ", %[kvlo], %[off]\n\ts_addc_u32 " PC1 ", %[kvhi], 0\n\t", "", "") \
  A_PERIOD(64, 80, KC, 120, 32, VC, 16, VC + 4, "", "", "", "s_waitcnt vmcnt(2)\n\t") \
  A_PERIOD(80, 64, KO, 96, 48, VC, 32, VC + 4, A_LD(KC, PC, 0) A_LD(KC + 4, PC, 1024), "", "", "s_add_u32 %[cnt], %[cnt], 1\n\t")

__global__ __launch_bounds__(320, 2) void k2_attn(const char* __restrict__ wsi, char* __restrict__ ws) {
  __shared__ __attribute__((aligned(1024))) char smem[K2_LDS];
  LDS_AS char* lds = (LDS_AS char*)smem;
  const int bid = blockIdx.x, xcd = bid & 7, jj = bid >> 3;
  const int head = 2 * xcd + (jj & 1), qb = jj >> 1;
  const int lane = threadIdx.x & 63, wave = __builtin_amdgcn_readfirstlane(threadIdx.x >> 6);
  const int h = lane >> 5;
  const int col = (jj >> 2) * 32 + xcd * 4 + (jj & 3);
  STAMP_DECL;
  if (wave == 4) {
    __builtin_amdgcn_s_sleep(A2_SLEEP);
    ST(0);
    if (col < DF) a2_column(col, lane, wsi, ws, lds + K2_TL);
    ST(1); STAMP_FLUSH(ws, 4);
    return;
  }
  ST(0);
  const int n0 = col < DF ? K2_N0 : 16, no = (64 - n0 + 2) / 3;
  const int st = wave == 0 ? 0 : n0 + (wave - 1) * no, nt = wave == 0 ? n0 : (wave == 3 ? 64 - st : no);
  const rsrc_t rkv = mkrsrc(wsi + WS_KV + head * 262144, 262144), rq = mkrsrc(wsi + WS_QF + (head * 64 + 4 * qb) * 2048, 8192);
  int offA = ((st + 4 * qb) & 63) << 12, offB = (offA + 4096) & 0x3ffff;
  const int l16 = lane * 16;
  u32x4 KA0 = bld(rkv, l16, offA), KA1 = bld(rkv, l16 + 1024, offA);
  u32x4 Q0[4], Q1[4];
#pragma unroll
  for (int t = 0; t < 4; ++t) { const int o = ((wave + t) & 3) * 2048; Q0[t] = bld(rq, l16, o); Q1[t] = bld(rq, l16 + 1024, o); }
  u32x4 VA0 = bld(rkv, l16 + 2048, offA), VA1 = bld(rkv, l16 + 3072, offA), KB0 = bld(rkv, l16, offB), KB1 = bld(rkv, l16 + 1024, offB);
  f32x16 O[4]; float mref[4];
#pragma unroll
  for (int t = 0; t < 4; ++t) {
    f32x16 s = MFMA(asb(KA0), asb(Q0[t]), zero16()); s = MFMA(asb(KA1), asb(Q1[t]), s);
    const float m = swapmax(max16(s));
    const unsigned nb = pk(-m, 0.f);
    mref[t] = -__uint_as_float(nb << 16);
    Q1[t][2] = h == 0 ? nb : Q1[t][2];
    O[t] = zero16();
  }
  ST(1);
  unsigned long long cy0 = CYC();
  u32x4 VB0, VB1 = {0, 0, 0, 0};
  {
    f32x16 SA = MFMA(asb(KA0), asb(Q0[0]), zero16()); SA = MFMA(asb(KA1), asb(Q1[0]), SA);
    int cnt = -nt;
    const uint64_t kvs = (uint64_t)(wsi + WS_KV + head * 262144);
    u32x4 P1 = {0, 0, 0, 0};
    asm volatile(
      "s_add_u32 s42, %[kvlo], %[off]\n\ts_addc_u32 s43, %[kvhi], 0\n\ts_nop 9\n"
      "Ltop%=:\n\t"
      A_HALF(128, 136, 144, 152, "s[40:41]", "s40", "s41", "s[42:43]")
      "s_cbranch_scc1 LendA%=\n\t"
      A_HALF(144, 152, 128, 136, "s[42:43]", "s42", "s43", "s[40:41]")
      "s_cbranch_scc0 Ltop%=\n\t"
      "s_nop 0\n\t"
      A_MF(48, 156, 164, A_ACC(48))
      "s_branch Ldone%=\n"
      "LendA%=:\n\t"
      "s_nop 0\n\t"
      A_MF(48, 140, 164, A_ACC(48))
      "Ldone%=:\n\t"
      "s_nop 11"
      : "+{v[0:15]}"(O[0]), "+{v[16:31]}"(O[1]), "+{v[32:47]}"(O[2]), "+{v[48:63]}"(O[3]), "+{v[64:79]}"(SA),
        "+{v[128:131]}"(KA0), "+{v[132:135]}"(KA1), "+{v[136:139]}"(VA0), "+{v[140:143]}"(VA1), "+{v[144:147]}"(KB0), "+{v[148:151]}"(KB1),
        "={v[152:155]}"(VB0), "+{v[156:159]}"(VB1), "+{v[164:167]}"(P1), [off] "+s"(offB), [cnt] "+s"(cnt)
      : "{v[96:99]}"(Q0[0]), "{v[100:103]}"(Q1[0]), "{v[104:107]}"(Q0[1]), "{v[108:111]}"(Q1[1]), "{v[112:115]}"(Q0[2]), "{v[116:119]}"(Q1[2]), "{v[120:123]}"(Q0[3]), "{v[124:127]}"(Q1[3]),
        "{v172}"(l16), [kvlo] "s"((unsigned)kvs), [kvhi] "s"((unsigned)(kvs >> 32))
      : "memory", "scc", "s40", "s41", "s42", "s43", "v80", "v81", "v82", "v83", "v84", "v85", "v86", "v87", "v88", "v89", "v90", "v91", "v92", "v93", "v94", "v95",
        "v160", "v161", "v162", "v163", "v168", "v169", "v170", "v171");
  }
  SC(5, cy0);
  ST(2);
#pragma unroll
  for (int t = 1; t < 4; ++t) {
    const int e = ((wave + t) & 3) * 3 + t - 1;
    LDS_AS char* p = lds + K2_EX + e * 4096 + l16;
#pragma unroll
    for (int g = 0; g < 4; ++g) *(LDS_AS f32x4*)(p + g * 1024) = (f32x4){O[t][4 * g], O[t][4 * g + 1], O[t][4 * g + 2], O[t][4 * g + 3]};
    *(LDS_AS float*)(lds + K2_MX + e * 256 + lane * 4) = mref[t];
  }
  asm volatile("s_waitcnt vmcnt(0) lgkmcnt(0)\n\ts_barrier"
               : "+{v[128:131]}"(KA0), "+{v[132:135]}"(KA1), "+{v[136:139]}"(VA0), "+{v[140:143]}"(VA1), "+{v[144:147]}"(KB0), "+{v[148:151]}"(KB1), "+{v[152:155]}"(VB0), "+{v[156:159]}"(VB1) :: "memory");
  ST(3);
  {
    f32x16 acc = O[0];
    float mw[3]; float M = mref[0];
#pragma unroll
    for (int k = 0; k < 3; ++k) { mw[k] = *(const LDS_AS float*)(lds + K2_MX + (wave * 3 + k) * 256 + lane * 4); M = fmaxf(M, mw[k]); }
    float lmin = acc[12], lmax = acc[12];
    const float fo = EX2(mref[0] - M);
#pragma unroll
    for (int i = 0; i < 16; ++i) acc[i] *= fo;
#pragma unroll
    for (int k = 0; k < 3; ++k) { const float f = EX2(mw[k] - M);
      const LDS_AS char* e = lds + K2_EX + (wave * 3 + k) * 4096 + l16;
#pragma unroll
      for (int g = 0; g < 4; ++g) { const f32x4 v = *(const LDS_AS f32x4*)(e + g * 1024);
        if (g == 3) { lmin = fminf(lmin, v[0]); lmax = fmaxf(lmax, v[0]); }
#pragma unroll
        for (int i = 0; i < 4; ++i) acc[4 * g + i] = fmaf(v[i], f, acc[4 * g + i]); } }
    float sm = 0.f;
#pragma unroll
    for (int i = 0; i < 16; ++i) sm += acc[i];
    const bool ok = (h == 1 || (lmin >= 0x1p-40f && lmax <= 0x1p40f)) && fabsf(sm) <= 3.0e38f;
    if (__builtin_expect(!__all(ok), 0)) {
      const bf16x8 q0 = asb(bld(rq, l16, wave * 2048)), q1 = asb(bld(rq, l16 + 1024, wave * 2048));
      float m = -INFINITY; acc = zero16();
      for (int kt = 0; kt < 64; ++kt) {
        f32x16 s = MFMA(asb(bld(rkv, l16, kt << 12)), q0, zero16()); s = MFMA(asb(bld(rkv, l16 + 1024, kt << 12)), q1, s);
        const float mn = fmaxf(m, swapmax(max16(s)));
        const float al = EX2(m - mn); m = mn;
#pragma unroll
        for (int i = 0; i < 16; ++i) { acc[i] *= al; s[i] = EX2(s[i] - mn); }
        acc = MFMA(asb(bld(rkv, l16 + 2048, kt << 12)), asb(pk8(s, 0)), acc); acc = MFMA(asb(bld(rkv, l16 + 3072, kt << 12)), asb(pk8(s, 1)), acc);
      }
    }
    auto rr = __builtin_amdgcn_permlane32_swap(__float_as_uint(acc[12]), __float_as_uint(acc[12]), false, false);
    const float inv = __builtin_amdgcn_rcpf(__uint_as_float(rr[0]));
#pragma unroll
    for (int i = 0; i < 16; ++i) acc[i] *= inv;
    u32x4 p0 = pk8(acc, 0), p1 = pk8(acc, 1);
    if (h == 0) { p1[2] = 0; p1[3] = 0; }
    char* dst = ws + WS_OF + ((4 * qb + wave) * 16 + head) * 2048 + l16;
    st16(dst, p0); st16(dst + 1024, p1);
  }
  ST(4);
  if (wave == 0) STAMP_FLUSH(ws, 1);
  if (wave == 1) STAMP_FLUSH(ws, 5);
}

__global__ __launch_bounds__(512, 2) void k3_out(const char* __restrict__ ws, const u16* __restrict__ wo, u16* __restrict__ out) {
  __shared__ __attribute__((aligned(1024))) char smem[57344];
  LDS_AS char* lds = (LDS_AS char*)smem;
  const int bid = blockIdx.x, xcd = bid & 7, jj = bid >> 3;
  const int part = xcd & 1, mt = 4 * (xcd >> 1) + (jj & 3), nt = jj >> 2;
  const int lane = threadIdx.x & 63, wave = __builtin_amdgcn_readfirstlane(threadIdx.x >> 6);
  if (wave >= 4) {
    const int lw = wave - 4, rr = lane >> 3, sl = lane & 7;
    const char* src; int pstride;
    if (part) { src = (const char*)wo + nt * 128 + rr * 896 + (((sl >> 2) ^ ((rr >> 1) & 1)) * 64) + (sl & 3) * 16 + lw * 8 * 896; pstride = 32 * 896; }
    else { src = nullptr; pstride = 0; }
#pragma unroll
    for (int i = 0; i < 14; ++i) {
      const int p = lw + 4 * i;
      const char* g;
      if (part) g = src + i * pstride;
      else { const int seg = p >> 3, row = 8 * (p & 7) + rr; g = (const char*)&g_F + (64 * nt + row) * 896 + seg * 128 + ((sl ^ ((row >> 1) & 7)) * 16); }
      dma16(g, lds + p * 1024);
    }
    asm volatile("s_waitcnt vmcnt(10)\n\ts_barrier" ::: "memory");
    asm volatile("s_waitcnt vmcnt(6)\n\ts_barrier" ::: "memory");
    asm volatile("s_waitcnt vmcnt(2)\n\ts_barrier" ::: "memory");
    asm volatile("s_waitcnt vmcnt(0)\n\ts_barrier" ::: "memory");
    return;
  }
  const int r = lane & 31, h = lane >> 5, tt = 4 * mt + wave;
  STAMP_DECL; ST(0);
  f32x16 acc0 = zero16(), acc1 = zero16();
  int tok, colb;
  if (part) {
    bf16x8 tk[32];
    const char* src = ws + WS_OF + tt * 32768 + lane * 16;
#pragma unroll
    for (int s = 0; s < 32; ++s) tk[s] = *(const bf16x8*)(src + s * 1024);
    const int q = (lane & 15) >> 2;
    const LDS_AS char* w0 = lds + (4 * h + q) * 128 + ((q >> 1) * 64) + ((lane >> 4) & 1) * 32 + (lane & 3) * 8;
    const LDS_AS char* w1 = lds + (4 * h + q) * 128 + (((q >> 1) ^ 1) * 64) + ((lane >> 4) & 1) * 32 + (lane & 3) * 8;
#pragma unroll
    for (int s = 0; s < 32; ++s) {
      if ((s & 7) == 0) asm volatile("s_barrier" ::: "memory");
      const int o = (s >> 1) * 3584 + (s & 1) * 2048, o2 = (s & 1) ? 512 : 1024;
      acc0 = MFMA(ldtr2(w0 + o, o2), tk[s], acc0); acc1 = MFMA(ldtr2(w1 + o, o2), tk[s], acc1);
    }
    tok = 32 * tt + r; colb = 448 + 64 * nt;
  } else {
    u32x4 tk[28];
    const char* src = ws + WS_Z2 + h * 4 * 8192 + (32 * tt + r) * 4;
#pragma unroll
    for (int s = 0; s < 28; ++s)
#pragma unroll
      for (int i = 0; i < 4; ++i) tk[s][i] = *(const unsigned*)(src + (8 * s + i) * 8192);
    const int sw = (r >> 1) & 7;
    const LDS_AS char* wa = lds + r * 128;
#pragma unroll
    for (int s = 0; s < 28; ++s) {
      if ((s & 7) == 0) asm volatile("s_barrier" ::: "memory");
      const LDS_AS char* p = wa + (s >> 2) * 8192 + (((2 * (s & 3) + h) ^ sw) * 16);
      acc0 = MFMA(ldsr(p), asb(tk[s]), acc0); acc1 = MFMA(ldsr(p + 4096), asb(tk[s]), acc1);
    }
    const int p = 32 * tt + r, l2 = (p >> 2) & 63, mg = p >> 8;
    tok = 64 * (l2 & 31) + 32 * (mg >> 2) + 8 * (mg & 3) + 4 * (l2 >> 5) + (p & 3); colb = 64 * nt;
  }
  ST(1);
  char* dst = (char*)out + tok * 1792 + colb * 2 + h * 16;
#pragma unroll
  for (int nb = 0; nb < 2; ++nb) {
    const f32x16& a = nb ? acc1 : acc0;
#pragma unroll
    for (int pr = 0; pr < 2; ++pr) {
      const unsigned a0 = pk(a[8 * pr], a[8 * pr + 1]), a1 = pk(a[8 * pr + 2], a[8 * pr + 3]), b0 = pk(a[8 * pr + 4], a[8 * pr + 5]), b1 = pk(a[8 * pr + 6], a[8 * pr + 7]);
      auto x = __builtin_amdgcn_permlane32_swap(a0, b0, false, false);
      auto y = __builtin_amdgcn_permlane32_swap(a1, b1, false, false);
      st16o(dst + nb * 64 + pr * 32, (u32x4){x[0], y[0], x[1], y[1]});
    }
  }
  ST(2);
  if (wave == 0) STAMP_FLUSH(ws, 2);
}

extern "C" void kernel_launch(void* const* d_in, const int* in_sizes, int n_in, void* d_out, int out_size, void* d_ws, size_t ws_size, hipStream_t stream) {
  char* ws = (char*)d_ws;
  k1_proj<<<256, 512, 0, stream>>>((const u16*)d_in[0], (const u16*)d_in[1], (const u16*)d_in[2], (const u16*)d_in[3], (const u16*)d_in[4], ws);
  k2_attn<<<256, 320, 0, stream>>>(ws, ws);
  k3_out<<<224, 512, 0, stream>>>(ws, (const u16*)d_in[5], (u16*)d_out);
}
```

```cpp
#include <hip/hip_runtime.h>
#include <stdint.h>

typedef unsigned short u16;
typedef short bf16x8 __attribute__((ext_vector_type(8)));
typedef short s16x4 __attribute__((ext_vector_type(4)));
typedef float f32x16 __attribute__((ext_vector_type(16)));
typedef float f32x4 __attribute__((ext_vector_type(4)));
typedef float f32x2 __attribute__((ext_vector_type(2)));
typedef unsigned u32x4 __attribute__((ext_vector_type(4)));
typedef unsigned u32x2 __attribute__((ext_vector_type(2)));
typedef __bf16 bf16x2_t __attribute__((ext_vector_type(2)));
#define LDS_AS __attribute__((address_space(3)))
#define DI __device__ __forceinline__
#define MFMA(a, b, c) __builtin_amdgcn_mfma_f32_32x32x16_bf16((a), (b), (c), 0, 0, 0)
#define SBAR() __builtin_amdgcn_sched_barrier(0)
#define PIN(x) asm volatile("" : "+v"(x))

constexpr int DF = 225;
#define K2_N0 13
#define K1_ROT 1
#define K1_PF 1
#define PRIO_M 0
#define PRIO_V 2
#define A2_SLEEP 8

namespace ct {
constexpr double PI = 3.14159265358979323846264338327950288;
constexpr double tsin(double x) { double x2 = x * x, t = x, s = x; for (int i = 1; i < 12; ++i) { t *= -x2 / ((2 * i) * (2 * i + 1)); s += t; } return s; }
constexpr double tcos(double x) { double x2 = x * x, t = 1, s = 1; for (int i = 1; i < 12; ++i) { t *= -x2 / ((2 * i - 1) * (2 * i)); s += t; } return s; }
constexpr double cos2pi(int j, int N) {
  j %= N; if (j < 0) j += N; if (2 * j > N) j = N - j; bool neg = false; if (4 * j > N) { j = N / 2 - j; neg = true; }
  double r = (8 * j > N) ? tsin(2 * PI * (N / 4 - j) / N) : tcos(2 * PI * j / N); return neg ? -r : r; }
constexpr double sin2pi(int j, int N) { return cos2pi(j - N / 4, N); }
constexpr u16 bf(double v) { float f = (float)v; unsigned u = __builtin_bit_cast(unsigned, f); u += 0x7fffu + ((u >> 16) & 1u); return (u16)(u >> 16); }
struct C448 { u16 c[448]; u16 ns[448]; };
constexpr C448 mk448() { C448 t{}; for (int j = 0; j < 448; ++j) { t.c[j] = bf(cos2pi(j, 448)); t.ns[j] = bf(-sin2pi(j, 448)); } return t; }
constexpr C448 T448 = mk448();
struct FPart { u16 v[56 * 448]; };
constexpr FPart mkF(int part) { FPart t{};
  for (int r = 0; r < 56; ++r) { int d = part * 56 + r; int a = 0; u16* o = &t.v[r * 448];
    for (int c = 0; c < 224; ++c) { o[2 * c] = T448.c[a]; o[2 * c + 1] = T448.ns[a]; a += d; if (a >= 448) a -= 448; }
    o[1] = (d & 1) ? 0xbf80 : 0x3f80; }
  return t; }
constexpr FPart F0 = mkF(0), F1 = mkF(1), F2 = mkF(2), F3 = mkF(3), F4 = mkF(4), F5 = mkF(5), F6 = mkF(6), F7 = mkF(7);
struct FTab { FPart p0, p1, p2, p3, p4, p5, p6, p7; u16 pad[128]; };
constexpr int kperm(int t, int h, int j) { return 16 * t + 8 * (j >> 2) + 4 * h + (j & 3); }
struct A2Tab {
  u16 t32f[3][2][64][8];
  u16 t64[3][2][4][64][8];
  u16 t32i[3][2][64][8];
};
constexpr A2Tab mkA2() { A2Tab t{};
  const double SC = 2.0 / (2048.0 * 448.0);
  for (int s = 0; s < 2; ++s) for (int l = 0; l < 64; ++l) for (int j = 0; j < 8; ++j) {
    int a = kperm(s, l >> 5, j) * (l & 31);
    t.t32f[0][s][l][j] = bf(cos2pi(a, 32)); t.t32f[1][s][l][j] = bf(sin2pi(a, 32)); t.t32f[2][s][l][j] = bf(-sin2pi(a, 32));
    int b = (16 * s + 8 * (l >> 5) + j) * (l & 31);
    t.t32i[0][s][l][j] = bf(SC * cos2pi(b, 32)); t.t32i[1][s][l][j] = bf(SC * sin2pi(b, 32)); t.t32i[2][s][l][j] = bf(-SC * sin2pi(b, 32)); }
  for (int m = 0; m < 2; ++m) for (int k = 0; k < 4; ++k) for (int l = 0; l < 64; ++l) for (int j = 0; j < 8; ++j) {
    int a = (32 * m + (l & 31)) * (32 * (k >> 1) + kperm(k & 1, l >> 5, j));
    t.t64[0][m][k][l][j] = bf(cos2pi(a, 64)); t.t64[1][m][k][l][j] = bf(sin2pi(a, 64)); t.t64[2][m][k][l][j] = bf(-sin2pi(a, 64)); }
  return t; }
struct TwTab { float tw[2][4][2][64][4]; };
constexpr TwTab mkTw() { TwTab t{};
  for (int m = 0; m < 2; ++m) for (int g = 0; g < 4; ++g) for (int l = 0; l < 64; ++l) for (int i = 0; i < 4; ++i) {
    int a = (32 * m + 8 * g + 4 * (l >> 5) + i) * (l & 31);
    t.tw[m][g][0][l][i] = (float)cos2pi(a, 2048); t.tw[m][g][1][l][i] = (float)sin2pi(a, 2048); }
  return t; }
}
__device__ __attribute__((aligned(256))) const ct::FTab g_F = { ct::F0, ct::F1, ct::F2, ct::F3, ct::F4, ct::F5, ct::F6, ct::F7, {} };
__device__ __attribute__((aligned(256))) const ct::A2Tab g_A2 = ct::mkA2();
__device__ __attribute__((aligned(256))) const ct::TwTab g_TW = ct::mkTw();

constexpr size_t WS_QF = 0;
constexpr size_t WS_KV = WS_QF + (2u << 20);
constexpr size_t WS_X1 = WS_KV + (4u << 20);
constexpr size_t WS_WT = WS_X1 + (2u << 20);
constexpr size_t WS_OF = WS_WT + (2u << 20);
constexpr size_t WS_Z2 = WS_OF + (2u << 20);

#define STAMP_DECL
#define ST(s) do {} while (0)
#define SC(s, base) do {} while (0)
#define CYC() 0ull
#define STAMP_FLUSH(wsp, k) do {} while (0)
DI unsigned pk(float a, float b) { f32x2 v = {a, b}; return __builtin_bit_cast(unsigned, __builtin_convertvector(v, bf16x2_t)); }
DI u32x4 pk8(const f32x16& x, int s) { u32x4 r; r[0] = pk(x[8 * s], x[8 * s + 1]); r[1] = pk(x[8 * s + 2], x[8 * s + 3]); r[2] = pk(x[8 * s + 4], x[8 * s + 5]); r[3] = pk(x[8 * s + 6], x[8 * s + 7]); return r; }
DI bf16x8 asb(u32x4 v) { return __builtin_bit_cast(bf16x8, v); }
DI bf16x8 ldsr(const LDS_AS char* p) { return *(const LDS_AS bf16x8*)p; }
DI s16x4 ldtr(const LDS_AS char* p) { return __builtin_amdgcn_ds_read_tr16_b64_v4i16((LDS_AS s16x4*)p); }
DI bf16x8 ldtr2(const LDS_AS char* p, int off2) { s16x4 lo = ldtr(p), hi = ldtr(p + off2); return __builtin_shufflevector(lo, hi, 0, 1, 2, 3, 4, 5, 6, 7); }
DI void dma16(const char* g, LDS_AS char* l) { __builtin_amdgcn_global_load_lds((const __attribute__((address_space(1))) unsigned*)g, (LDS_AS unsigned*)l, 16, 0, 0); }
#define WAITV(n) asm volatile("s_waitcnt vmcnt(" #n ")" ::: "memory")
#define BARRIER() asm volatile("s_waitcnt lgkmcnt(0)\n\ts_barrier" ::: "memory")
DI const f32x16 zero16() { f32x16 z; for (int i = 0; i < 16; ++i) z[i] = 0.f; return z; }

typedef __amdgpu_buffer_rsrc_t rsrc_t;
DI rsrc_t mkrsrc(const void* p, unsigned bytes) { return __builtin_amdgcn_make_buffer_rsrc((void*)p, 0, bytes, 0x00020000); }
DI u32x4 bld(rsrc_t r, int voff, int soff) { return __builtin_amdgcn_raw_buffer_load_b128(r, voff, soff, 0); }

constexpr int K1_SLOT = 30720;
template <bool TRANS, int WROW>
DI void k1_loop(const LDS_AS char* lds, int lane, int wm, int wn, f32x16 (&acc)[2][2], unsigned long long* sto) {
  constexpr int SLOT = 16384 + 64 * WROW, BLK = WROW / 4;
  const int r = lane & 31, h = lane >> 5;
  const int sw = (r >> 1) & 7;
  const LDS_AS char* xa = lds + (wm * 64 + r) * 128;
  int xo[4];
#pragma unroll
  for (int s = 0; s < 4; ++s) xo[s] = ((2 * s + h) ^ sw) * 16;
  const int q = (lane & 15) >> 2, c4 = ((lane >> 4) & 1) * 4 + (lane & 3);
  const LDS_AS char *wb0, *wb1; int t1;
  if (WROW == 224) {
    wb0 = lds + 16384 + (8 * h + 2 * q) * 224 + 2 * wn * 56 + (c4 >= 6 ? c4 - 1 : c4) * 8; wb1 = wb0 + 56; t1 = 224;
  } else {
    wb0 = lds + 16384 + (8 * h + q) * 256 + (((2 * wn) ^ q) * 64) + c4 * 8; wb1 = lds + 16384 + (8 * h + q) * 256 + (((2 * wn + 1) ^ q) * 64) + c4 * 8; t1 = 1024;
  }
  bf16x8 a0, a1, b0, b1, na0, na1, nb0, nb1;
  PIN(acc[0][0]); PIN(acc[0][1]); PIN(acc[1][0]); PIN(acc[1][1]);
  STAMP_DECL; unsigned long long c0 = CYC();
  BARRIER();
  SC(0, c0);
  a0 = ldsr(xa + xo[0]); b0 = ldtr2(wb0, t1); b1 = ldtr2(wb1, t1); a1 = ldsr(xa + xo[0] + 4096);
  int so = 0;
#define K1_MF(i, j, A, B) acc[i][j] = TRANS ? MFMA(B, A, acc[i][j]) : MFMA(A, B, acc[i][j])
#pragma unroll 1
  for (int c = 0; c < 7; ++c) {
#pragma unroll
    for (int s = 0; s < 4; ++s) {
      int xoff, woff;
      if (s < 3) { xoff = so + xo[s + 1]; woff = so + (s + 1) * 16 * WROW; }
      else {
        if (c < 6) BARRIER();
        SC(c + 1, c0);
        so = (so == 3 * SLOT) ? 0 : so + SLOT;
        xoff = so + xo[0]; woff = so;
      }
      SBAR();
      K1_MF(0, 0, a0, b0); SBAR();
      na0 = ldsr(xa + xoff); nb0 = ldtr2(wb0 + woff, t1); SBAR();
      K1_MF(0, 1, a0, b1); SBAR();
      nb1 = ldtr2(wb1 + woff, t1); na1 = ldsr(xa + xoff + 4096); SBAR();
      K1_MF(1, 0, a1, b0); SBAR();
      K1_MF(1, 1, a1, b1); SBAR();
      a0 = na0; a1 = na1; b0 = nb0; b1 = nb1;
    }
  }
}

template <int NW>
DI void k1_loader(LDS_AS char* lds, int lane, int lw, int bid, int mt, int nt, const u16* ax, const u16* wr, const u16* wi, const u16* bx, const u16* wqkv, char* ws) {
  const bool spec = nt >= 12;
  const char* xb = (const char*)(spec ? ax : bx);
  const char* wbase = spec ? (const char*)&g_F + (nt - 12) * 256 : (const char*)wqkv + nt * 224;
  const int ldb = spec ? 896 : 2688, slot = spec ? 32768 : 30720;
  unsigned vx[4], vw[4];
  int wp0;
#pragma unroll
  for (int i = 0; i < 4; ++i) {
    const int row = 8 * (lw * 4 + i) + (lane >> 3), kg = (lane & 7) ^ ((row >> 1) & 7);
    const int tok = spec ? 64 * (row & 31) + 4 * mt + (row >> 5) : 128 * mt + row;
    vx[i] = tok * 896 + kg * 16;
  }
  if (spec) {
    wp0 = lw * 4;
#pragma unroll
    for (int i = 0; i < 4; ++i) { const int wrow = 4 * (wp0 + i) + (lane >> 4); vw[i] = wrow * 896 + (((lane >> 2) & 3) ^ (wrow & 3)) * 64 + (lane & 3) * 16; }
  } else {
    wp0 = lw < 2 ? lw * 4 : 8 + (lw - 2) * 3;
#pragma unroll
    for (int i = 0; i < NW; ++i) { const int idx = 64 * (wp0 + i) + lane, pos = (idx * 4682) >> 16, pc = idx - 14 * pos;
      const int k = (pos & ~7) | ((pos & 1) << 2) | ((pos >> 1) & 3); vw[i] = k * 2688 + pc * 16; }
  }
  const int rot = K1_ROT ? (2 * (nt & 3) + (mt & 7)) % 7 : 0;
  auto issue = [&](int c) {
    const int cs = c + rot >= 7 ? c + rot - 7 : c + rot;
    const char* xs = xb + cs * 128; const char* wsrc = wbase + cs * 64 * ldb;
    LDS_AS char* l = lds + (c & 3) * slot;
#pragma unroll
    for (int i = 0; i < 4; ++i) dma16(xs + vx[i], l + (lw * 4 + i) * 1024);
#pragma unroll
    for (int i = 0; i < NW; ++i) dma16(wsrc + vw[i], l + 16384 + (wp0 + i) * 1024);
  };
  issue(0); issue(1); issue(2);
  unsigned wv[2][8]; int wc[2], wf1[2];
  const int a = bid >> 4;
  { const int o = (bid >> 2) & 3, cg = bid & 3;
#pragma unroll
    for (int k = 0; k < 2; ++k) {
      int u = lw * 64 + lane + 256 * k; u = u > 455 ? 455 : u;
      const int f1l = (u * 1150) >> 16, cl = u - 57 * f1l;
      int c = 57 * cg + cl; c = c > 224 ? 224 : c;
      wc[k] = c; wf1[k] = 8 * o + f1l;
      const int off = ((128 * a + 8 * o + f1l) * DF + c) * 2;
#pragma unroll
      for (int i = 0; i < 4; ++i) {
        asm volatile("global_load_ushort %0, %1, %2" : "=v"(wv[k][2 * i]) : "v"(off + i * 32 * DF * 2), "s"(wr));
        asm volatile("global_load_ushort %0, %1, %2" : "=v"(wv[k][2 * i + 1]) : "v"(off + i * 32 * DF * 2), "s"(wi));
      }
    } }
  constexpr int P = 4 + NW;
#define K1_WB(n) asm volatile("s_waitcnt vmcnt(%0)\n\ts_barrier" :: "i"(n) : "memory")
  K1_WB(2 * P + 16);
  issue(3);
  K1_WB(2 * P + 16);
  issue(4);
  K1_WB(2 * P + 16);
  issue(5);
  asm volatile("s_waitcnt vmcnt(%16)\n\ts_barrier"
               : "+v"(wv[0][0]), "+v"(wv[0][1]), "+v"(wv[0][2]), "+v"(wv[0][3]), "+v"(wv[0][4]), "+v"(wv[0][5]), "+v"(wv[0][6]), "+v"(wv[0][7]),
                 "+v"(wv[1][0]), "+v"(wv[1][1]), "+v"(wv[1][2]), "+v"(wv[1][3]), "+v"(wv[1][4]), "+v"(wv[1][5]), "+v"(wv[1][6]), "+v"(wv[1][7]) : "i"(2 * P) : "memory");
  issue(6);
  K1_WB(2 * P);
  K1_WB(P);
  K1_WB(0);
#pragma unroll
  for (int k = 0; k < 2; ++k) {
    u32x4 o;
    const bool half = (wc[k] == 0) || (wc[k] == 224);
#pragma unroll
    for (int i = 0; i < 4; ++i) {
      unsigned re = wv[k][2 * i] << 16, im = wv[k][2 * i + 1] << 16;
      if (half) { re = __float_as_uint(__uint_as_float(re) * 0.5f); im = __float_as_uint(__uint_as_float(im) * 0.5f); }
      o[i] = (re >> 16) | (im & 0xffff0000u);
    }
    *(u32x4*)(ws + WS_WT + wc[k] * 8192 + ((a >> 1) * 64 + wf1[k] + 32 * (a & 1)) * 16) = o;
  }
}

__global__ __launch_bounds__(512, 2) void k1_proj(const u16* __restrict__ ax, const u16* __restrict__ wr, const u16* __restrict__ wi,
                                                  const u16* __restrict__ bx, const u16* __restrict__ wqkv, char* __restrict__ ws) {
  __shared__ __attribute__((aligned(1024))) char smem[4 * 32768];
  LDS_AS char* lds = (LDS_AS char*)smem;
  const int bid = blockIdx.x, xcd = bid & 7, jj = bid >> 3;
  const int mt = 8 * (xcd & 1) + (jj & 7), nt = 4 * (xcd >> 1) + (jj >> 3), type = nt >> 2;
  const int lane = threadIdx.x & 63, wave = __builtin_amdgcn_readfirstlane(threadIdx.x >> 6);
  if (wave >= 4) {
    if (type == 3 || wave < 6) k1_loader<4>(lds, lane, wave - 4, bid, mt, nt, ax, wr, wi, bx, wqkv, ws);
    else k1_loader<3>(lds, lane, wave - 4, bid, mt, nt, ax, wr, wi, bx, wqkv, ws);
    return;
  }
  if (bid == 0 && threadIdx.x == 0) *(unsigned long long*)(ws + (32u << 20)) = __builtin_amdgcn_s_memrealtime();
  const int wm = wave & 1, wn = wave >> 1, r = lane & 31, h = lane >> 5;
  STAMP_DECL; unsigned long long sto[8]; ST(0);
  unsigned pf0 = 0, pf1 = 0;
  if (K1_PF) {
    int i = wave * 64 + lane; i = i > 223 ? 223 : i;
    const int L = (nt & 3) * 224 + i, row = (L * 9363) >> 16, seg = L - 7 * row;
    const int tok = type == 3 ? 64 * (row & 31) + 4 * mt + (row >> 5) : 128 * mt + row;
    asm volatile("global_load_dword %0, %1, %2" : "=v"(pf0) : "v"(tok * 896 + seg * 128), "s"(type == 3 ? ax : bx));
    int j = wave * 64 + lane; j = j > 167 ? 167 : j;
    const int wrow = (j * 21846) >> 16, ls = j - 3 * wrow;
    const int woff = (56 * (mt & 7) + wrow) * (type == 3 ? 896 : 2688) + (ls == 2 ? 220 : ls * 128);
    asm volatile("global_load_dword %0, %1, %2" : "=v"(pf1) : "v"(woff), "s"(type == 3 ? (const char*)&g_F + (nt - 12) * 256 : (const char*)wqkv + nt * 224));
  }
  f32x16 acc[2][2];
#pragma unroll
  for (int i = 0; i < 2; ++i)
#pragma unroll
    for (int j = 0; j < 2; ++j) acc[i][j] = zero16();
  if (type < 2) k1_loop<true, 224>(lds, lane, wm, wn, acc, sto); else if (type == 2) k1_loop<false, 224>(lds, lane, wm, wn, acc, sto); else k1_loop<false, 256>(lds, lane, wm, wn, acc, sto);
  asm volatile("s_waitcnt vmcnt(0)" : "+v"(pf0), "+v"(pf1));
  ST(1);
  const float qs = type == 0 ? 0.18898223650461362f * 1.4426950408889634f : 1.0f;
  const bool padqk = type < 2 && h == 0, padv = type == 2 && (r >> 2) == 6;
  const unsigned kone = type == 1 ? 0x3f80u : 0u, vone = r == 24 ? 0x3f803f80u : 0u;
  char* dst0 = type == 0 ? ws + WS_QF + (4 * nt * 64) * 2048 : type == 3 ? ws + WS_X1 + (4 * (nt - 12) * 64) * 2048 : ws + WS_KV + (4 * (nt & 3) * 64) * 4096 + (type == 2 ? 2048 : 0);
  const int tsz = (type == 0 || type == 3) ? 2048 : 4096;
  dst0 += (2 * wn * 64 + 4 * mt + 2 * wm) * tsz + lane * 16;
  if (nt == 15 && wn == 1) return;
#pragma unroll
  for (int mi = 0; mi < 2; ++mi)
#pragma unroll
    for (int ni = 0; ni < 2; ++ni) {
      f32x16 v = acc[mi][ni];
#pragma unroll
      for (int i = 0; i < 16; ++i) v[i] *= qs;
      u32x4 p0 = pk8(v, 0), p1 = pk8(v, 1);
      if (padqk) { p1[2] = kone; p1[3] = 0; }
      if (padv) { p0 = (u32x4){vone, vone, vone, vone}; p1 = p0; }
      char* dst = dst0 + (ni * 64 + mi) * tsz;
      *(u32x4*)dst = p0; *(u32x4*)(dst + 1024) = p1;
    }
  ST(2);
}

DI float swapmax(float m) { auto rr = __builtin_amdgcn_permlane32_swap(__float_as_uint(m), __float_as_uint(m), false, false); return fmaxf(__uint_as_float(rr[0]), __uint_as_float(rr[1])); }
DI float max16(const f32x16& s) {
  float a = fmaxf(fmaxf(s[0], s[1]), s[2]), b = fmaxf(fmaxf(s[3], s[4]), s[5]);
  a = fmaxf(fmaxf(a, s[6]), s[7]); b = fmaxf(fmaxf(b, s[8]), s[9]); a = fmaxf(fmaxf(a, s[10]), s[11]); b = fmaxf(fmaxf(b, s[12]), s[13]);
  a = fmaxf(fmaxf(a, s[14]), s[15]); return fmaxf(a, b); }
#define EX2(x) __builtin_amdgcn_exp2f(x)

template <bool CONJ>
DI void cmul2(float a0, float b0, float c0, float s0, float a1, float b1, float c1, float s1, unsigned& re, unsigned& im) {
  float t0, t1, t2, t3;
  if (CONJ)
    asm("v_mul_f32 %2, %7, %9\n\tv_mul_f32 %3, %6, %9\n\tv_mul_f32 %4, %11, %13\n\tv_mul_f32 %5, %10, %13\n\t"
        "v_fma_f32 %2, %6, %8, %2\n\tv_fma_f32 %3, %7, %8, -%3\n\tv_fma_f32 %4, %10, %12, %4\n\tv_fma_f32 %5, %11, %12, -%5\n\t"
        "v_cvt_pk_bf16_f32 %0, %2, %4\n\tv_cvt_pk_bf16_f32 %1, %3, %5"
        : "=v"(re), "=v"(im), "=&v"(t0), "=&v"(t1), "=&v"(t2), "=&v"(t3) : "v"(a0), "v"(b0), "v"(c0), "v"(s0), "v"(a1), "v"(b1), "v"(c1), "v"(s1));
  else
    asm("v_mul_f32 %2, %7, %9\n\tv_mul_f32 %3, %6, %9\n\tv_mul_f32 %4, %11, %13\n\tv_mul_f32 %5, %10, %13\n\t"
        "v_fma_f32 %2, %6, %8, -%2\n\tv_fma_f32 %3, %7, %8, %3\n\tv_fma_f32 %4, %10, %12, -%4\n\tv_fma_f32 %5, %11, %12, %5\n\t"
        "v_cvt_pk_bf16_f32 %0, %2, %4\n\tv_cvt_pk_bf16_f32 %1, %3, %5"
        : "=v"(re), "=v"(im), "=&v"(t0), "=&v"(t1), "=&v"(t2), "=&v"(t3) : "v"(a0), "v"(b0), "v"(c0), "v"(s0), "v"(a1), "v"(b1), "v"(c1), "v"(s1));
}
#define MF_DONE(x, y, z, w) do { __builtin_amdgcn_s_setprio(PRIO_V); asm volatile("s_nop 11" : "+v"(x), "+v"(y), "+v"(z), "+v"(w)); SBAR(); } while (0)
#define VA_DONE(b) do { __builtin_amdgcn_s_setprio(PRIO_M); asm volatile("s_nop 1" : "+v"(b[0]), "+v"(b[1]), "+v"(b[2]), "+v"(b[3]), "+v"(b[4]), "+v"(b[5]), "+v"(b[6]), "+v"(b[7])); SBAR(); } while (0)

DI void a2_column(int c, int lane, const char* __restrict__ ws_in, char* __restrict__ ws, LDS_AS char* tl) {
  const int r = lane & 31, h = lane >> 5, l16 = lane * 16;
  const bool real_col = (c == 0) || (c == 224);
  const int cp = c == 224 ? 1 : 2 * c;
  const rsrc_t rx = mkrsrc(ws_in + WS_X1 + (cp >> 5) * 131072 + (cp & 31) * 16, 0x40000u);
  const rsrc_t rt = mkrsrc(&g_A2, sizeof(g_A2)), rw = mkrsrc(&g_TW, sizeof(g_TW)), rf = mkrsrc(ws_in + WS_WT + c * 8192, 8192);
  const int xv = r * 2048 + h * 512;
#define T32F(mat, t) asb(bld(rt, l16, ((mat) * 2 + (t)) * 1024))
#define T64(mat, m, k) asb(bld(rt, l16, 6144 + (((mat) * 2 + (m)) * 4 + (k)) * 1024))
#define T32I(mat, t) asb(bld(rt, l16, 6144 + 24576 + ((mat) * 2 + (t)) * 1024))
#define TW(m, g, p) __builtin_bit_cast(f32x4, bld(rw, l16, (((m) * 4 + (g)) * 2 + (p)) * 1024))
  bf16x8 ur[2][2], ui[2][2], t32[3][2], t64[3][2][4]; f32x4 tw[2][4][2]; u32x4 wv[2][4];
#pragma unroll
  for (int m = 0; m < 2; ++m)
#pragma unroll
    for (int t = 0; t < 2; ++t) { ur[m][t] = asb(bld(rx, xv, m * 65536 + t * 1024)); ui[m][t] = asb(bld(rx, xv, m * 65536 + t * 1024 + 16)); }
#pragma unroll
  for (int i = 0; i < 6; ++i) t32[i >> 1][i & 1] = T32F(i >> 1, i & 1);
#pragma unroll
  for (int i = 0; i < 16; ++i) tw[i >> 3][(i >> 1) & 3][i & 1] = TW(i >> 3, (i >> 1) & 3, i & 1);
  SBAR();
  if (real_col) {
#pragma unroll
    for (int m = 0; m < 2; ++m)
#pragma unroll
      for (int t = 0; t < 2; ++t) ui[m][t] = (bf16x8){0, 0, 0, 0, 0, 0, 0, 0};
  }
  __builtin_amdgcn_s_setprio(PRIO_M);
  f32x16 re[2], im[2];
#pragma unroll
  for (int m = 0; m < 2; ++m) {
    re[m] = MFMA(ur[m][0], t32[0][0], zero16()); im[m] = MFMA(ui[m][0], t32[0][0], zero16());
    re[m] = MFMA(ur[m][1], t32[0][1], re[m]);    im[m] = MFMA(ui[m][1], t32[0][1], im[m]);
    re[m] = MFMA(ui[m][0], t32[1][0], re[m]);    im[m] = MFMA(ur[m][0], t32[2][0], im[m]);
    re[m] = MFMA(ui[m][1], t32[1][1], re[m]);    im[m] = MFMA(ur[m][1], t32[2][1], im[m]);
  }
  SBAR();
#pragma unroll
  for (int i = 0; i < 12; ++i) t64[i >> 2][0][i & 3] = T64(i >> 2, 0, i & 3);
  MF_DONE(re[0], im[0], re[1], im[1]);
  u32x4 b[8];
#pragma unroll
  for (int m = 0; m < 2; ++m)
#pragma unroll
    for (int e = 0; e < 16; e += 2) { const f32x4 cs = tw[m][e >> 2][0], sn = tw[m][e >> 2][1]; unsigned pr, pi;
      cmul2<true>(re[m][e], im[m][e], cs[e & 3], sn[e & 3], re[m][e + 1], im[m][e + 1], cs[(e & 3) + 1], sn[(e & 3) + 1], pr, pi);
      b[2 * m + (e >> 3)][(e >> 1) & 3] = pr; b[4 + 2 * m + (e >> 3)][(e >> 1) & 3] = pi; }
  SBAR();
#pragma unroll
  for (int i = 0; i < 12; ++i) t64[i >> 2][1][i & 3] = T64(i >> 2, 1, i & 3);
#pragma unroll
  for (int i = 0; i < 8; ++i) wv[i >> 2][i & 3] = bld(rf, l16, i * 1024);
  VA_DONE(b);
#pragma unroll
  for (int m = 0; m < 2; ++m) {
    re[m] = zero16(); im[m] = zero16();
#pragma unroll
    for (int k = 0; k < 4; ++k) {
      re[m] = MFMA(t64[0][m][k], asb(b[k]), re[m]); im[m] = MFMA(t64[0][m][k], asb(b[4 + k]), im[m]);
      re[m] = MFMA(t64[1][m][k], asb(b[4 + k]), re[m]); im[m] = MFMA(t64[2][m][k], asb(b[k]), im[m]);
    }
  }
  SBAR();
  { int l2 = l16; asm volatile("" : "+v"(l2));
#pragma unroll
    for (int i = 0; i < 12; ++i) t64[i >> 2][0][i & 3] = asb(bld(rt, l2, 6144 + ((i >> 2) * 8 + (i & 3)) * 1024)); }
  MF_DONE(re[0], im[0], re[1], im[1]);
  int l3 = l16; asm volatile("" : "+v"(l3));
#pragma unroll
  for (int m = 0; m < 2; ++m)
#pragma unroll
    for (int e = 0; e < 16; e += 2) { const unsigned w0 = wv[m][e >> 2][e & 3], w1 = wv[m][e >> 2][(e & 3) + 1]; unsigned pr, pi;
      cmul2<false>(re[m][e], im[m][e], __uint_as_float(w0 << 16), __uint_as_float(w0 & 0xffff0000u), re[m][e + 1], im[m][e + 1], __uint_as_float(w1 << 16), __uint_as_float(w1 & 0xffff0000u), pr, pi);
      b[2 * m + (e >> 3)][(e >> 1) & 3] = pr; b[4 + 2 * m + (e >> 3)][(e >> 1) & 3] = pi; }
  SBAR();
#pragma unroll
  for (int i = 0; i < 8; ++i) tw[0][i >> 1][i & 1] = __builtin_bit_cast(f32x4, bld(rw, l3, i * 1024));
  VA_DONE(b);
#pragma unroll
  for (int m = 0; m < 2; ++m) {
    re[m] = zero16(); im[m] = zero16();
#pragma unroll
    for (int k = 0; k < 4; ++k) {
      re[m] = MFMA(t64[0][m][k], asb(b[k]), re[m]); im[m] = MFMA(t64[0][m][k], asb(b[4 + k]), im[m]);
      re[m] = MFMA(t64[2][m][k], asb(b[4 + k]), re[m]); im[m] = MFMA(t64[1][m][k], asb(b[k]), im[m]);
    }
    SBAR();
    if (m == 0) {
#pragma unroll
      for (int i = 0; i < 8; ++i) tw[1][i >> 1][i & 1] = __builtin_bit_cast(f32x4, bld(rw, l3, (8 + i) * 1024));
      SBAR();
    }
  }
#pragma unroll
  for (int i = 0; i < 6; ++i) t32[i >> 1][i & 1] = T32I(i >> 1, i & 1);
  MF_DONE(re[0], im[0], re[1], im[1]);
#pragma unroll
  for (int m = 0; m < 2; ++m)
#pragma unroll
    for (int g = 0; g < 4; ++g) { const f32x4 cs = tw[m][g][0], sn = tw[m][g][1]; u32x2 pr, pi; unsigned x, y;
      cmul2<false>(re[m][4 * g], im[m][4 * g], cs[0], sn[0], re[m][4 * g + 1], im[m][4 * g + 1], cs[1], sn[1], x, y); pr[0] = x; pi[0] = y;
      cmul2<false>(re[m][4 * g + 2], im[m][4 * g + 2], cs[2], sn[2], re[m][4 * g + 3], im[m][4 * g + 3], cs[3], sn[3], x, y); pr[1] = x; pi[1] = y;
      LDS_AS char* p = tl + r * 192 + (32 * m + 8 * g + 4 * h) * 2;
      *(LDS_AS u32x2*)p = pr; *(LDS_AS u32x2*)(p + 6144) = pi;
    }
  __builtin_amdgcn_s_setprio(PRIO_M);
  asm volatile("s_waitcnt lgkmcnt(0)" ::: "memory");
  const LDS_AS char* tp = tl + (8 * h + ((lane & 15) >> 2)) * 192 + ((lane >> 4) & 1) * 32 + (lane & 3) * 8;
#pragma unroll
  for (int m = 0; m < 2; ++m) {
    re[m] = zero16(); im[m] = zero16();
#pragma unroll
    for (int t = 0; t < 2; ++t) {
      const bf16x8 ar = ldtr2(tp + t * 16 * 192 + m * 64, 4 * 192), ai = ldtr2(tp + 6144 + t * 16 * 192 + m * 64, 4 * 192);
      re[m] = MFMA(ar, t32[0][t], re[m]); im[m] = MFMA(ai, t32[0][t], im[m]);
      re[m] = MFMA(ai, t32[2][t], re[m]); im[m] = MFMA(ar, t32[1][t], im[m]);
    }
  }
  SBAR(); __builtin_amdgcn_s_setprio(PRIO_V);
  if (!real_col) {
    char* z = ws + WS_Z2 + c * 8192 + lane * 16;
#pragma unroll
    for (int m = 0; m < 2; ++m)
#pragma unroll
      for (int g = 0; g < 4; ++g)
        *(u32x4*)(z + (m * 4 + g) * 1024) = (u32x4){pk(re[m][4 * g], im[m][4 * g]), pk(re[m][4 * g + 1], im[m][4 * g + 1]), pk(re[m][4 * g + 2], im[m][4 * g + 2]), pk(re[m][4 * g + 3], im[m][4 * g + 3])};
  } else {
    char* z = ws + WS_Z2 + lane * 16 + (c ? 2 : 0);
#pragma unroll
    for (int m = 0; m < 2; ++m)
#pragma unroll
      for (int g = 0; g < 16; ++g) *(u16*)(z + (m * 4 + (g >> 2)) * 1024 + (g & 3) * 4) = (u16)pk(re[m][g], 0.f);
  }
}

constexpr int K2_EX = 0, K2_MX = 49152, K2_TL = K2_MX + 3072, K2_LDS = K2_TL + 12288;

#define A_MF(d, a, b, c) "v_mfma_f32_32x32x16_bf16 v[" #d ":" #d "+15], v[" #a ":" #a "+3], v[" #b ":" #b "+3], " c "\n\t"
#define A_ACC(d) "v[" #d ":" #d "+15]"
#define A_EXP(s, p, X) X "v_exp_f32 v168, v[" #s "]\n\tv_exp_f32 v169, v[" #s "+1]\n\tv_exp_f32 v170, v[" #s "+2]\n\tv_exp_f32 v171, v[" #s "+3]\n\t" \
                    "v_cvt_pk_bf16_f32 v[" #p "], v168, v169\n\tv_cvt_pk_bf16_f32 v[" #p "+1], v170, v171\n\t"
#define A_PERIOD(SC, SN, KN, QN, OC, V0C, OP, V1P, XA, XB, XC, XD) \
  A_MF(SN, KN, QN, "0")                 A_EXP(SC, 160, XA) \
  A_MF(OP, V1P, 164, A_ACC(OP))         A_EXP(SC + 4, 162, XB) \
  A_MF(SN, KN + 4, QN + 4, A_ACC(SN))   A_EXP(SC + 8, 164, XC) \
  A_MF(OC, V0C, 160, A_ACC(OC))         A_EXP(SC + 12, 166, XD)
#define A_LD(d, so, off) "global_load_dwordx4 v[" #d ":" #d "+3], v172, " so " offset:" #off "\n\t"
#define A_HALF(KC, VC, KO, VO, PC, PC0, PC1, PO)   \
  A_PERIOD(64, 80, KC, 104, 0, VC, 48, VO + 4, "", A_LD(VO, PO, 2048) A_LD(VO + 4, PO, 3072), "s_waitcnt vmcnt(4)\n\t", "") \
  A_PERIOD(80, 64, KC, 112, 16, VC, 0, VC + 4, "s_add_u32 %[off], %[off], 0x1000\n\ts_and_b32 %[off], %[off], 0x3ffff\n\t", "s_add_u32 " PC0 ", %[kvlo], %[off]\n\ts_addc_u32 " PC1 ", %[kvhi], 0\n\t", "", "") \
  A_PERIOD(64, 80, KC, 120, 32, VC, 16, VC + 4, "", "", "", "s_waitcnt vmcnt(2)\n\t") \
  A_PERIOD(80, 64, KO, 96, 48, VC, 32, VC + 4, A_LD(KC, PC, 0) A_LD(KC + 4, PC, 1024), "", "", "s_add_u32 %[cnt], %[cnt], 1\n\t")

__global__ __launch_bounds__(320, 2) void k2_attn(const char* __restrict__ wsi, char* __restrict__ ws) {
  __shared__ __attribute__((aligned(1024))) char smem[K2_LDS];
  LDS_AS char* lds = (LDS_AS char*)smem;
  const int bid = blockIdx.x, xcd = bid & 7, jj = bid >> 3;
  const int head = 2 * xcd + (jj & 1), qb = jj >> 1;
  const int lane = threadIdx.x & 63, wave = __builtin_amdgcn_readfirstlane(threadIdx.x >> 6);
  const int h = lane >> 5;
  const int col = (jj >> 2) * 32 + xcd * 4 + (jj & 3);
  STAMP_DECL;
  if (wave == 4) {
    __builtin_amdgcn_s_sleep(A2_SLEEP);
    ST(0);
    if (col < DF) a2_column(col, lane, wsi, ws, lds + K2_TL);
    ST(1); STAMP_FLUSH(ws, 4);
    return;
  }
  ST(0);
  const int n0 = col < DF ? K2_N0 : 16, no = (64 - n0 + 2) / 3;
  const int st = wave == 0 ? 0 : n0 + (wave - 1) * no, nt = wave == 0 ? n0 : (wave == 3 ? 64 - st : no);
  const rsrc_t rkv = mkrsrc(wsi + WS_KV + head * 262144, 262144), rq = mkrsrc(wsi + WS_QF + (head * 64 + 4 * qb) * 2048, 8192);
  int offA = ((st + 4 * qb) & 63) << 12, offB = (offA + 4096) & 0x3ffff;
  const int l16 = lane * 16;
  u32x4 KA0 = bld(rkv, l16, offA), KA1 = bld(rkv, l16 + 1024, offA);
  u32x4 Q0[4], Q1[4];
#pragma unroll
  for (int t = 0; t < 4; ++t) { const int o = ((wave + t) & 3) * 2048; Q0[t] = bld(rq, l16, o); Q1[t] = bld(rq, l16 + 1024, o); }
  u32x4 VA0 = bld(rkv, l16 + 2048, offA), VA1 = bld(rkv, l16 + 3072, offA), KB0 = bld(rkv, l16, offB), KB1 = bld(rkv, l16 + 1024, offB);
  f32x16 O[4]; float mref[4];
#pragma unroll
  for (int t = 0; t < 4; ++t) {
    f32x16 s = MFMA(asb(KA0), asb(Q0[t]), zero16()); s = MFMA(asb(KA1), asb(Q1[t]), s);
    const float m = swapmax(max16(s));
    const unsigned nb = pk(-m, 0.f);
    mref[t] = -__uint_as_float(nb << 16);
    Q1[t][2] = h == 0 ? nb : Q1[t][2];
    O[t] = zero16();
  }
  ST(1);
  unsigned long long cy0 = CYC();
  u32x4 VB0, VB1 = {0, 0, 0, 0};
  {
    f32x16 SA = MFMA(asb(KA0), asb(Q0[0]), zero16()); SA = MFMA(asb(KA1), asb(Q1[0]), SA);
    int cnt = -nt;
    const uint64_t kvs = (uint64_t)(wsi + WS_KV + head * 262144);
    u32x4 P1 = {0, 0, 0, 0};
    asm volatile(
      "s_add_u32 s42, %[kvlo], %[off]\n\ts_addc_u32 s43, %[kvhi], 0\n\ts_nop 9\n"
      "Ltop%=:\n\t"
      A_HALF(128, 136, 144, 152, "s[40:41]", "s40", "s41", "s[42:43]")
      "s_cbranch_scc1 LendA%=\n\t"
      A_HALF(144, 152, 128, 136, "s[42:43]", "s42", "s43", "s[40:41]")
      "s_cbranch_scc0 Ltop%=\n\t"
      "s_nop 0\n\t"
      A_MF(48, 156, 164, A_ACC(48))
      "s_branch Ldone%=\n"
      "LendA%=:\n\t"
      "s_nop 0\n\t"
      A_MF(48, 140, 164, A_ACC(48))
      "Ldone%=:\n\t"
      "s_nop 11"
      : "+{v[0:15]}"(O[0]), "+{v[16:31]}"(O[1]), "+{v[32:47]}"(O[2]), "+{v[48:63]}"(O[3]), "+{v[64:79]}"(SA),
        "+{v[128:131]}"(KA0), "+{v[132:135]}"(KA1), "+{v[136:139]}"(VA0), "+{v[140:143]}"(VA1), "+{v[144:147]}"(KB0), "+{v[148:151]}"(KB1),
        "={v[152:155]}"(VB0), "+{v[156:159]}"(VB1), "+{v[164:167]}"(P1), [off] "+s"(offB), [cnt] "+s"(cnt)
      : "{v[96:99]}"(Q0[0]), "{v[100:103]}"(Q1[0]), "{v[104:107]}"(Q0[1]), "{v[108:111]}"(Q1[1]), "{v[112:115]}"(Q0[2]), "{v[116:119]}"(Q1[2]), "{v[120:123]}"(Q0[3]), "{v[124:127]}"(Q1[3]),
        "{v172}"(l16), [kvlo] "s"((unsigned)kvs), [kvhi] "s"((unsigned)(kvs >> 32))
      : "memory", "scc", "s40", "s41", "s42", "s43", "v80", "v81", "v82", "v83", "v84", "v85", "v86", "v87", "v88", "v89", "v90", "v91", "v92", "v93", "v94", "v95",
        "v160", "v161", "v162", "v163", "v168", "v169", "v170", "v171");
  }
  SC(5, cy0);
  ST(2);
#pragma unroll
  for (int t = 1; t < 4; ++t) {
    const int e = ((wave + t) & 3) * 3 + t - 1;
    LDS_AS char* p = lds + K2_EX + e * 4096 + l16;
#pragma unroll
    for (int g = 0; g < 4; ++g) *(LDS_AS f32x4*)(p + g * 1024) = (f32x4){O[t][4 * g], O[t][4 * g + 1], O[t][4 * g + 2], O[t][4 * g + 3]};
    *(LDS_AS float*)(lds + K2_MX + e * 256 + lane * 4) = mref[t];
  }
  asm volatile("s_waitcnt vmcnt(0) lgkmcnt(0)\n\ts_barrier"
               : "+{v[128:131]}"(KA0), "+{v[132:135]}"(KA1), "+{v[136:139]}"(VA0), "+{v[140:143]}"(VA1), "+{v[144:147]}"(KB0), "+{v[148:151]}"(KB1), "+{v[152:155]}"(VB0), "+{v[156:159]}"(VB1) :: "memory");
  ST(3);
  {
    f32x16 acc = O[0];
    float mw[3]; float M = mref[0];
#pragma unroll
    for (int k = 0; k < 3; ++k) { mw[k] = *(const LDS_AS float*)(lds + K2_MX + (wave * 3 + k) * 256 + lane * 4); M = fmaxf(M, mw[k]); }
    float lmin = acc[12], lmax = acc[12];
    const float fo = EX2(mref[0] - M);
#pragma unroll
    for (int i = 0; i < 16; ++i) acc[i] *= fo;
#pragma unroll
    for (int k = 0; k < 3; ++k) { const float f = EX2(mw[k] - M);
      const LDS_AS char* e = lds + K2_EX + (wave * 3 + k) * 4096 + l16;
#pragma unroll
      for (int g = 0; g < 4; ++g) { const f32x4 v = *(const LDS_AS f32x4*)(e + g * 1024);
        if (g == 3) { lmin = fminf(lmin, v[0]); lmax = fmaxf(lmax, v[0]); }
#pragma unroll
        for (int i = 0; i < 4; ++i) acc[4 * g + i] = fmaf(v[i], f, acc[4 * g + i]); } }
    float sm = 0.f;
#pragma unroll
    for (int i = 0; i < 16; ++i) sm += acc[i];
    const bool ok = (h == 1 || (lmin >= 0x1p-40f && lmax <= 0x1p40f)) && fabsf(sm) <= 3.0e38f;
    if (__builtin_expect(!__all(ok), 0)) {
      const bf16x8 q0 = asb(bld(rq, l16, wave * 2048)), q1 = asb(bld(rq, l16 + 1024, wave * 2048));
      float m = -INFINITY; acc = zero16();
      for (int kt = 0; kt < 64; ++kt) {
        f32x16 s = MFMA(asb(bld(rkv, l16, kt << 12)), q0, zero16()); s = MFMA(asb(bld(rkv, l16 + 1024, kt << 12)), q1, s);
        const float mn = fmaxf(m, swapmax(max16(s)));
        const float al = EX2(m - mn); m = mn;
#pragma unroll
        for (int i = 0; i < 16; ++i) { acc[i] *= al; s[i] = EX2(s[i] - mn); }
        acc = MFMA(asb(bld(rkv, l16 + 2048, kt << 12)), asb(pk8(s, 0)), acc); acc = MFMA(asb(bld(rkv, l16 + 3072, kt << 12)), asb(pk8(s, 1)), acc);
      }
    }
    auto rr = __builtin_amdgcn_permlane32_swap(__float_as_uint(acc[12]), __float_as_uint(acc[12]), false, false);
    const float inv = __builtin_amdgcn_rcpf(__uint_as_float(rr[0]));
#pragma unroll
    for (int i = 0; i < 16; ++i) acc[i] *= inv;
    u32x4 p0 = pk8(acc, 0), p1 = pk8(acc, 1);
    if (h == 0) { p1[2] = 0; p1[3] = 0; }
    char* dst = ws + WS_OF + ((4 * qb + wave) * 16 + head) * 2048 + l16;
    *(u32x4*)dst = p0; *(u32x4*)(dst + 1024) = p1;
  }
  ST(4);
  if (wave == 0) STAMP_FLUSH(ws, 1);
  if (wave == 1) STAMP_FLUSH(ws, 5);
}

__global__ __launch_bounds__(512, 2) void k3_out(const char* __restrict__ ws, const u16* __restrict__ wo, u16* __restrict__ out) {
  __shared__ __attribute__((aligned(1024))) char smem[57344];
  LDS_AS char* lds = (LDS_AS char*)smem;
  const int bid = blockIdx.x, xcd = bid & 7, jj = bid >> 3;
  const int part = xcd & 1, mt = 4 * (xcd >> 1) + (jj & 3), nt = jj >> 2;
  const int lane = threadIdx.x & 63, wave = __builtin_amdgcn_readfirstlane(threadIdx.x >> 6);
  if (wave >= 4) {
    const int lw = wave - 4, rr = lane >> 3, sl = lane & 7;
    const char* src; int pstride;
    if (part) { src = (const char*)wo + nt * 128 + rr * 896 + (((sl >> 2) ^ ((rr >> 1) & 1)) * 64) + (sl & 3) * 16 + lw * 8 * 896; pstride = 32 * 896; }
    else { src = nullptr; pstride = 0; }
#pragma unroll
    for (int i = 0; i < 14; ++i) {
      const int p = lw + 4 * i;
      const char* g;
      if (part) g = src + i * pstride;
      else { const int seg = p >> 3, row = 8 * (p & 7) + rr; g = (const char*)&g_F + (64 * nt + row) * 896 + seg * 128 + ((sl ^ ((row >> 1) & 7)) * 16); }
      dma16(g, lds + p * 1024);
    }
    asm volatile("s_waitcnt vmcnt(10)\n\ts_barrier" ::: "memory");
    asm volatile("s_waitcnt vmcnt(6)\n\ts_barrier" ::: "memory");
    asm volatile("s_waitcnt vmcnt(2)\n\ts_barrier" ::: "memory");
    asm volatile("s_waitcnt vmcnt(0)\n\ts_barrier" ::: "memory");
    return;
  }
  const int r = lane & 31, h = lane >> 5, tt = 4 * mt + wave;
  STAMP_DECL; ST(0);
  f32x16 acc0 = zero16(), acc1 = zero16();
  int tok, colb;
  if (part) {
    bf16x8 tk[32];
    const char* src = ws + WS_OF + tt * 32768 + lane * 16;
#pragma unroll
    for (int s = 0; s < 32; ++s) tk[s] = *(const bf16x8*)(src + s * 1024);
    const int q = (lane & 15) >> 2;
    const LDS_AS char* w0 = lds + (4 * h + q) * 128 + ((q >> 1) * 64) + ((lane >> 4) & 1) * 32 + (lane & 3) * 8;
    const LDS_AS char* w1 = lds + (4 * h + q) * 128 + (((q >> 1) ^ 1) * 64) + ((lane >> 4) & 1) * 32 + (lane & 3) * 8;
#pragma unroll
    for (int s = 0; s < 32; ++s) {
      if ((s & 7) == 0) asm volatile("s_barrier" ::: "memory");
      const int o = (s >> 1) * 3584 + (s & 1) * 2048, o2 = (s & 1) ? 512 : 1024;
      acc0 = MFMA(ldtr2(w0 + o, o2), tk[s], acc0); acc1 = MFMA(ldtr2(w1 + o, o2), tk[s], acc1);
    }
    tok = 32 * tt + r; colb = 448 + 64 * nt;
  } else {
    u32x4 tk[28];
    const char* src = ws + WS_Z2 + h * 4 * 8192 + (32 * tt + r) * 4;
#pragma unroll
    for (int s = 0; s < 28; ++s)
#pragma unroll
      for (int i = 0; i < 4; ++i) tk[s][i] = *(const unsigned*)(src + (8 * s + i) * 8192);
    const int sw = (r >> 1) & 7;
    const LDS_AS char* wa = lds + r * 128;
#pragma unroll
    for (int s = 0; s < 28; ++s) {
      if ((s & 7) == 0) asm volatile("s_barrier" ::: "memory");
      const LDS_AS char* p = wa + (s >> 2) * 8192 + (((2 * (s & 3) + h) ^ sw) * 16);
      acc0 = MFMA(ldsr(p), asb(tk[s]), acc0); acc1 = MFMA(ldsr(p + 4096), asb(tk[s]), acc1);
    }
    const int p = 32 * tt + r, l2 = (p >> 2) & 63, mg = p >> 8;
    tok = 64 * (l2 & 31) + 32 * (mg >> 2) + 8 * (mg & 3) + 4 * (l2 >> 5) + (p & 3); colb = 64 * nt;
  }
  ST(1);
  char* dst = (char*)out + tok * 1792 + colb * 2 + h * 16;
#pragma unroll
  for (int nb = 0; nb < 2; ++nb) {
    const f32x16& a = nb ? acc1 : acc0;
#pragma unroll
    for (int pr = 0; pr < 2; ++pr) {
      const unsigned a0 = pk(a[8 * pr], a[8 * pr + 1]), a1 = pk(a[8 * pr + 2], a[8 * pr + 3]), b0 = pk(a[8 * pr + 4], a[8 * pr + 5]), b1 = pk(a[8 * pr + 6], a[8 * pr + 7]);
      auto x = __builtin_amdgcn_permlane32_swap(a0, b0, false, false);
      auto y = __builtin_amdgcn_permlane32_swap(a1, b1, false, false);
      *(u32x4*)(dst + nb * 64 + pr * 32) = (u32x4){x[0], y[0], x[1], y[1]};
    }
  }
  ST(2);
  { const unsigned long long t0 = *(const volatile unsigned long long*)(ws + (32u << 20)); for (int i = 0; i < 200000; ++i) { if (__builtin_amdgcn_s_memrealtime() - t0 >= 4500ull) break; __builtin_amdgcn_s_sleep(1); } }
}

extern "C" void kernel_launch(void* const* d_in, const int* in_sizes, int n_in, void* d_out, int out_size, void* d_ws, size_t ws_size, hipStream_t stream) {
  char* ws = (char*)d_ws;
  k1_proj<<<256, 512, 0, stream>>>((const u16*)d_in[0], (const u16*)d_in[1], (const u16*)d_in[2], (const u16*)d_in[3], (const u16*)d_in[4], ws);
  k2_attn<<<256, 320, 0, stream>>>(ws, ws);
  k3_out<<<224, 512, 0, stream>>>(ws, (const u16*)d_in[5], (u16*)d_out);
}
```

```cpp
#include <hip/hip_runtime.h>
#include <stdint.h>

typedef unsigned short u16;
typedef short bf16x8 __attribute__((ext_vector_type(8)));
typedef short s16x4 __attribute__((ext_vector_type(4)));
typedef float f32x16 __attribute__((ext_vector_type(16)));
typedef float f32x4 __attribute__((ext_vector_type(4)));
typedef float f32x2 __attribute__((ext_vector_type(2)));
typedef unsigned u32x4 __attribute__((ext_vector_type(4)));
typedef unsigned u32x2 __attribute__((ext_vector_type(2)));
typedef __bf16 bf16x2_t __attribute__((ext_vector_type(2)));
#define LDS_AS __attribute__((address_space(3)))
#define DI __device__ __forceinline__
#define MFMA(a, b, c) __builtin_amdgcn_mfma_f32_32x32x16_bf16((a), (b), (c), 0, 0, 0)
#define SBAR() __builtin_amdgcn_sched_barrier(0)
#define PIN(x) asm volatile("" : "+v"(x))

constexpr int DF = 225;
#define K2_N0 13
#define K1_ROT 1
#define K1_PF 1
#define PRIO_M 0
#define PRIO_V 2
#define A2_SLEEP 8

namespace ct {
constexpr double PI = 3.14159265358979323846264338327950288;
constexpr double tsin(double x) { double x2 = x * x, t = x, s = x; for (int i = 1; i < 12; ++i) { t *= -x2 / ((2 * i) * (2 * i + 1)); s += t; } return s; }
constexpr double tcos(double x) { double x2 = x * x, t = 1, s = 1; for (int i = 1; i < 12; ++i) { t *= -x2 / ((2 * i - 1) * (2 * i)); s += t; } return s; }
constexpr double cos2pi(int j, int N) {
  j %= N; if (j < 0) j += N; if (2 * j > N) j = N - j; bool neg = false; if (4 * j > N) { j = N / 2 - j; neg = true; }
  double r = (8 * j > N) ? tsin(2 * PI * (N / 4 - j) / N) : tcos(2 * PI * j / N); return neg ? -r : r; }
constexpr double sin2pi(int j, int N) { return cos2pi(j - N / 4, N); }
constexpr u16 bf(double v) { float f = (float)v; unsigned u = __builtin_bit_cast(unsigned, f); u += 0x7fffu + ((u >> 16) & 1u); return (u16)(u >> 16); }
struct C448 { u16 c[448]; u16 ns[448]; };
constexpr C448 mk448() { C448 t{}; for (int j = 0; j < 448; ++j) { t.c[j] = bf(cos2pi(j, 448)); t.ns[j] = bf(-sin2pi(j, 448)); } return t; }
constexpr C448 T448 = mk448();
struct FPart { u16 v[56 * 448]; };
constexpr FPart mkF(int part) { FPart t{};
  for (int r = 0; r < 56; ++r) { int d = part * 56 + r; int a = 0; u16* o = &t.v[r * 448];
    for (int c = 0; c < 224; ++c) { o[2 * c] = T448.c[a]; o[2 * c + 1] = T448.ns[a]; a += d; if (a >= 448) a -= 448; }
    o[1] = (d & 1) ? 0xbf80 : 0x3f80; }
  return t; }
constexpr FPart F0 = mkF(0), F1 = mkF(1), F2 = mkF(2), F3 = mkF(3), F4 = mkF(4), F5 = mkF(5), F6 = mkF(6), F7 = mkF(7);
struct FTab { FPart p0, p1, p2, p3, p4, p5, p6, p7; u16 pad[128]; };
constexpr int kperm(int t, int h, int j) { return 16 * t + 8 * (j >> 2) + 4 * h + (j & 3); }
struct A2Tab {
  u16 t32f[3][2][64][8];
  u16 t64[3][2][4][64][8];
  u16 t32i[3][2][64][8];
};
constexpr A2Tab mkA2() { A2Tab t{};
  const double SC = 2.0 / (2048.0 * 448.0);
  for (int s = 0; s < 2; ++s) for (int l = 0; l < 64; ++l) for (int j = 0; j < 8; ++j) {
    int a = kperm(s, l >> 5, j) * (l & 31);
    t.t32f[0][s][l][j] = bf(cos2pi(a, 32)); t.t32f[1][s][l][j] = bf(sin2pi(a, 32)); t.t32f[2][s][l][j] = bf(-sin2pi(a, 32));
    int b = (16 * s + 8 * (l >> 5) + j) * (l & 31);
    t.t32i[0][s][l][j] = bf(SC * cos2pi(b, 32)); t.t32i[1][s][l][j] = bf(SC * sin2pi(b, 32)); t.t32i[2][s][l][j] = bf(-SC * sin2pi(b, 32)); }
  for (int m = 0; m < 2; ++m) for (int k = 0; k < 4; ++k) for (int l = 0; l < 64; ++l) for (int j = 0; j < 8; ++j) {
    int a = (32 * m + (l & 31)) * (32 * (k >> 1) + kperm(k & 1, l >> 5, j));
    t.t64[0][m][k][l][j] = bf(cos2pi(a, 64)); t.t64[1][m][k][l][j] = bf(sin2pi(a, 64)); t.t64[2][m][k][l][j] = bf(-sin2pi(a, 64)); }
  return t; }
struct TwTab { float tw[2][4][2][64][4]; };
constexpr TwTab mkTw() { TwTab t{};
  for (int m = 0; m < 2; ++m) for (int g = 0; g < 4; ++g) for (int l = 0; l < 64; ++l) for (int i = 0; i < 4; ++i) {
    int a = (32 * m + 8 * g + 4 * (l >> 5) + i) * (l & 31);
    t.tw[m][g][0][l][i] = (float)cos2pi(a, 2048); t.tw[m][g][1][l][i] = (float)sin2pi(a, 2048); }
  return t; }
}
__device__ __attribute__((aligned(256))) const ct::FTab g_F = { ct::F0, ct::F1, ct::F2, ct::F3, ct::F4, ct::F5, ct::F6, ct::F7, {} };
__device__ __attribute__((aligned(256))) const ct::A2Tab g_A2 = ct::mkA2();
__device__ __attribute__((aligned(256))) const ct::TwTab g_TW = ct::mkTw();

constexpr size_t WS_QF = 0;
constexpr size_t WS_KV = WS_QF + (2u << 20);
constexpr size_t WS_X1 = WS_KV + (4u << 20);
constexpr size_t WS_WT = WS_X1 + (2u << 20);
constexpr size_t WS_OF = WS_WT + (2u << 20);
constexpr size_t WS_Z2 = WS_OF + (2u << 20);

#define STAMP_DECL
#define ST(s) do {} while (0)
#define SC(s, base) do {} while (0)
#define CYC() 0ull
#define STAMP_FLUSH(wsp, k) do {} while (0)
DI unsigned pk(float a, float b) { f32x2 v = {a, b}; return __builtin_bit_cast(unsigned, __builtin_convertvector(v, bf16x2_t)); }
DI u32x4 pk8(const f32x16& x, int s) { u32x4 r; r[0] = pk(x[8 * s], x[8 * s + 1]); r[1] = pk(x[8 * s + 2], x[8 * s + 3]); r[2] = pk(x[8 * s + 4], x[8 * s + 5]); r[3] = pk(x[8 * s + 6], x[8 * s + 7]); return r; }
DI bf16x8 asb(u32x4 v) { return __builtin_bit_cast(bf16x8, v); }
DI bf16x8 ldsr(const LDS_AS char* p) { return *(const LDS_AS bf16x8*)p; }
DI s16x4 ldtr(const LDS_AS char* p) { return __builtin_amdgcn_ds_read_tr16_b64_v4i16((LDS_AS s16x4*)p); }
DI bf16x8 ldtr2(const LDS_AS char* p, int off2) { s16x4 lo = ldtr(p), hi = ldtr(p + off2); return __builtin_shufflevector(lo, hi, 0, 1, 2, 3, 4, 5, 6, 7); }
DI void dma16(const char* g, LDS_AS char* l) { __builtin_amdgcn_global_load_lds((const __attribute__((address_space(1))) unsigned*)g, (LDS_AS unsigned*)l, 16, 0, 0); }
#define WAITV(n) asm volatile("s_waitcnt vmcnt(" #n ")" ::: "memory")
#define BARRIER() asm volatile("s_waitcnt lgkmcnt(0)\n\ts_barrier" ::: "memory")
DI const f32x16 zero16() { f32x16 z; for (int i = 0; i < 16; ++i) z[i] = 0.f; return z; }

#define ST_POL ""
#define OUT_POL ""
DI void st16(void* p, u32x4 v) { asm volatile("global_store_dwordx4 %0, %1, off " ST_POL "\n\ts_nop 1" :: "v"(p), "v"(v) : "memory"); }
DI void st16o(void* p, u32x4 v) { asm volatile("global_store_dwordx4 %0, %1, off " OUT_POL "\n\ts_nop 1" :: "v"(p), "v"(v) : "memory"); }
typedef __amdgpu_buffer_rsrc_t rsrc_t;
DI rsrc_t mkrsrc(const void* p, unsigned bytes) { return __builtin_amdgcn_make_buffer_rsrc((void*)p, 0, bytes, 0x00020000); }
DI u32x4 bld(rsrc_t r, int voff, int soff) { return __builtin_amdgcn_raw_buffer_load_b128(r, voff, soff, 0); }

constexpr int K1_SLOT = 30720;
template <bool TRANS, int WROW>
DI void k1_loop(const LDS_AS char* lds, int lane, int wm, int wn, f32x16 (&acc)[2][2], unsigned long long* sto) {
  constexpr int SLOT = 16384 + 64 * WROW, BLK = WROW / 4;
  const int r = lane & 31, h = lane >> 5;
  const int sw = (r >> 1) & 7;
  const LDS_AS char* xa = lds + (wm * 64 + r) * 128;
  int xo[4];
#pragma unroll
  for (int s = 0; s < 4; ++s) xo[s] = ((2 * s + h) ^ sw) * 16;
  const int q = (lane & 15) >> 2, c4 = ((lane >> 4) & 1) * 4 + (lane & 3);
  const LDS_AS char *wb0, *wb1; int t1;
  if (WROW == 224) {
    wb0 = lds + 16384 + (8 * h + 2 * q) * 224 + 2 * wn * 56 + (c4 >= 6 ? c4 - 1 : c4) * 8; wb1 = wb0 + 56; t1 = 224;
  } else {
    wb0 = lds + 16384 + (8 * h + q) * 256 + (((2 * wn) ^ q) * 64) + c4 * 8; wb1 = lds + 16384 + (8 * h + q) * 256 + (((2 * wn + 1) ^ q) * 64) + c4 * 8; t1 = 1024;
  }
  bf16x8 a0, a1, b0, b1, na0, na1, nb0, nb1;
  PIN(acc[0][0]); PIN(acc[0][1]); PIN(acc[1][0]); PIN(acc[1][1]);
  STAMP_DECL; unsigned long long c0 = CYC();
  BARRIER();
  SC(0, c0);
  a0 = ldsr(xa + xo[0]); b0 = ldtr2(wb0, t1); b1 = ldtr2(wb1, t1); a1 = ldsr(xa + xo[0] + 4096);
  int so = 0;
#define K1_MF(i, j, A, B) acc[i][j] = TRANS ? MFMA(B, A, acc[i][j]) : MFMA(A, B, acc[i][j])
#pragma unroll 1
  for (int c = 0; c < 7; ++c) {
#pragma unroll
    for (int s = 0; s < 4; ++s) {
      int xoff, woff;
      if (s < 3) { xoff = so + xo[s + 1]; woff = so + (s + 1) * 16 * WROW; }
      else {
        if (c < 6) BARRIER();
        SC(c + 1, c0);
        so = (so == 3 * SLOT) ? 0 : so + SLOT;
        xoff = so + xo[0]; woff = so;
      }
      SBAR();
      K1_MF(0, 0, a0, b0); SBAR();
      na0 = ldsr(xa + xoff); nb0 = ldtr2(wb0 + woff, t1); SBAR();
      K1_MF(0, 1, a0, b1); SBAR();
      nb1 = ldtr2(wb1 + woff, t1); na1 = ldsr(xa + xoff + 4096); SBAR();
      K1_MF(1, 0, a1, b0); SBAR();
      K1_MF(1, 1, a1, b1); SBAR();
      a0 = na0; a1 = na1; b0 = nb0; b1 = nb1;
    }
  }
}

template <int NW>
DI void k1_loader(LDS_AS char* lds, int lane, int lw, int bid, int mt, int nt, const u16* ax, const u16* wr, const u16* wi, const u16* bx, const u16* wqkv, char* ws) {
  const bool spec = nt >= 12;
  const char* xb = (const char*)(spec ? ax : bx);
  const char* wbase = spec ? (const char*)&g_F + (nt - 12) * 256 : (const char*)wqkv + nt * 224;
  const int ldb = spec ? 896 : 2688, slot = spec ? 32768 : 30720;
  unsigned vx[4], vw[4];
  int wp0;
#pragma unroll
  for (int i = 0; i < 4; ++i) {
    const int row = 8 * (lw * 4 + i) + (lane >> 3), kg = (lane & 7) ^ ((row >> 1) & 7);
    const int tok = spec ? 64 * (row & 31) + 4 * mt + (row >> 5) : 128 * mt + row;
    vx[i] = tok * 896 + kg * 16;
  }
  if (spec) {
    wp0 = lw * 4;
#pragma unroll
    for (int i = 0; i < 4; ++i) { const int wrow = 4 * (wp0 + i) + (lane >> 4); vw[i] = wrow * 896 + (((lane >> 2) & 3) ^ (wrow & 3)) * 64 + (lane & 3) * 16; }
  } else {
    wp0 = lw < 2 ? lw * 4 : 8 + (lw - 2) * 3;
#pragma unroll
    for (int i = 0; i < NW; ++i) { const int idx = 64 * (wp0 + i) + lane, pos = (idx * 4682) >> 16, pc = idx - 14 * pos;
      const int k = (pos & ~7) | ((pos & 1) << 2) | ((pos >> 1) & 3); vw[i] = k * 2688 + pc * 16; }
  }
  const int rot = K1_ROT ? (2 * (nt & 3) + (mt & 7)) % 7 : 0;
  auto issue = [&](int c) {
    const int cs = c + rot >= 7 ? c + rot - 7 : c + rot;
    const char* xs = xb + cs * 128; const char* wsrc = wbase + cs * 64 * ldb;
    LDS_AS char* l = lds + (c & 3) * slot;
#pragma unroll
    for (int i = 0; i < 4; ++i) dma16(xs + vx[i], l + (lw * 4 + i) * 1024);
#pragma unroll
    for (int i = 0; i < NW; ++i) dma16(wsrc + vw[i], l + 16384 + (wp0 + i) * 1024);
  };
  issue(0); issue(1); issue(2);
  unsigned wv[2][8]; int wc[2], wf1[2];
  const int a = bid >> 4;
  { const int o = (bid >> 2) & 3, cg = bid & 3;
#pragma unroll
    for (int k = 0; k < 2; ++k) {
      int u = lw * 64 + lane + 256 * k; u = u > 455 ? 455 : u;
      const int f1l = (u * 1150) >> 16, cl = u - 57 * f1l;
      int c = 57 * cg + cl; c = c > 224 ? 224 : c;
      wc[k] = c; wf1[k] = 8 * o + f1l;
      const int off = ((128 * a + 8 * o + f1l) * DF + c) * 2;
#pragma unroll
      for (int i = 0; i < 4; ++i) {
        asm volatile("global_load_ushort %0, %1, %2" : "=v"(wv[k][2 * i]) : "v"(off + i * 32 * DF * 2), "s"(wr));
        asm volatile("global_load_ushort %0, %1, %2" : "=v"(wv[k][2 * i + 1]) : "v"(off + i * 32 * DF * 2), "s"(wi));
      }
    } }
  constexpr int P = 4 + NW;
#define K1_WB(n) asm volatile("s_waitcnt vmcnt(%0)\n\ts_barrier" :: "i"(n) : "memory")
  K1_WB(2 * P + 16);
  issue(3);
  K1_WB(2 * P + 16);
  issue(4);
  K1_WB(2 * P + 16);
  issue(5);
  asm volatile("s_waitcnt vmcnt(%16)\n\ts_barrier"
               : "+v"(wv[0][0]), "+v"(wv[0][1]), "+v"(wv[0][2]), "+v"(wv[0][3]), "+v"(wv[0][4]), "+v"(wv[0][5]), "+v"(wv[0][6]), "+v"(wv[0][7]),
                 "+v"(wv[1][0]), "+v"(wv[1][1]), "+v"(wv[1][2]), "+v"(wv[1][3]), "+v"(wv[1][4]), "+v"(wv[1][5]), "+v"(wv[1][6]), "+v"(wv[1][7]) : "i"(2 * P) : "memory");
  issue(6);
  K1_WB(2 * P);
  K1_WB(P);
  K1_WB(0);
#pragma unroll
  for (int k = 0; k < 2; ++k) {
    u32x4 o;
    const bool half = (wc[k] == 0) || (wc[k] == 224);
#pragma unroll
    for (int i = 0; i < 4; ++i) {
      unsigned re = wv[k][2 * i] << 16, im = wv[k][2 * i + 1] << 16;
      if (half) { re = __float_as_uint(__uint_as_float(re) * 0.5f); im = __float_as_uint(__uint_as_float(im) * 0.5f); }
      o[i] = (re >> 16) | (im & 0xffff0000u);
    }
    st16(ws + WS_WT + wc[k] * 8192 + ((a >> 1) * 64 + wf1[k] + 32 * (a & 1)) * 16, o);
  }
}

__global__ __launch_bounds__(512, 2) void k1_proj(const u16* __restrict__ ax, const u16* __restrict__ wr, const u16* __restrict__ wi,
                                                  const u16* __restrict__ bx, const u16* __restrict__ wqkv, char* __restrict__ ws) {
  __shared__ __attribute__((aligned(1024))) char smem[4 * 32768];
  LDS_AS char* lds = (LDS_AS char*)smem;
  const int bid = blockIdx.x, xcd = bid & 7, jj = bid >> 3;
  const int mt = 8 * (xcd & 1) + (jj & 7), nt = 4 * (xcd >> 1) + (jj >> 3), type = nt >> 2;
  const int lane = threadIdx.x & 63, wave = __builtin_amdgcn_readfirstlane(threadIdx.x >> 6);
  if (wave >= 4) {
    if (type == 3 || wave < 6) k1_loader<4>(lds, lane, wave - 4, bid, mt, nt, ax, wr, wi, bx, wqkv, ws);
    else k1_loader<3>(lds, lane, wave - 4, bid, mt, nt, ax, wr, wi, bx, wqkv, ws);
    return;
  }
  const int wm = wave & 1, wn = wave >> 1, r = lane & 31, h = lane >> 5;
  STAMP_DECL; unsigned long long sto[8]; ST(0);
  unsigned pf0 = 0, pf1 = 0;
  if (K1_PF) {
    int i = wave * 64 + lane; i = i > 223 ? 223 : i;
    const int L = (nt & 3) * 224 + i, row = (L * 9363) >> 16, seg = L - 7 * row;
    const int tok = type == 3 ? 64 * (row & 31) + 4 * mt + (row >> 5) : 128 * mt + row;
    asm volatile("global_load_dword %0, %1, %2" : "=v"(pf0) : "v"(tok * 896 + seg * 128), "s"(type == 3 ? ax : bx));
    int j = wave * 64 + lane; j = j > 167 ? 167 : j;
    const int wrow = (j * 21846) >> 16, ls = j - 3 * wrow;
    const int woff = (56 * (mt & 7) + wrow) * (type == 3 ? 896 : 2688) + (ls == 2 ? 220 : ls * 128);
    asm volatile("global_load_dword %0, %1, %2" : "=v"(pf1) : "v"(woff), "s"(type == 3 ? (const char*)&g_F + (nt - 12) * 256 : (const char*)wqkv + nt * 224));
  }
  f32x16 acc[2][2];
#pragma unroll
  for (int i = 0; i < 2; ++i)
#pragma unroll
    for (int j = 0; j < 2; ++j) acc[i][j] = zero16();
  if (type < 2) k1_loop<true, 224>(lds, lane, wm, wn, acc, sto); else if (type == 2) k1_loop<false, 224>(lds, lane, wm, wn, acc, sto); else k1_loop<false, 256>(lds, lane, wm, wn, acc, sto);
  asm volatile("s_waitcnt vmcnt(0)" : "+v"(pf0), "+v"(pf1));
  ST(1);
  const float qs = type == 0 ? 0.18898223650461362f * 1.4426950408889634f : 1.0f;
  const bool padqk = type < 2 && h == 0, padv = type == 2 && (r >> 2) == 6;
  const unsigned kone = type == 1 ? 0x3f80u : 0u, vone = r == 24 ? 0x3f803f80u : 0u;
  char* dst0 = type == 0 ? ws + WS_QF + (4 * nt * 64) * 2048 : type == 3 ? ws + WS_X1 + (4 * (nt - 12) * 64) * 2048 : ws + WS_KV + (4 * (nt & 3) * 64) * 4096 + (type == 2 ? 2048 : 0);
  const int tsz = (type == 0 || type == 3) ? 2048 : 4096;
  dst0 += (2 * wn * 64 + 4 * mt + 2 * wm) * tsz + lane * 16;
  if (nt == 15 && wn == 1) return;
#pragma unroll
  for (int mi = 0; mi < 2; ++mi)
#pragma unroll
    for (int ni = 0; ni < 2; ++ni) {
      f32x16 v = acc[mi][ni];
#pragma unroll
      for (int i = 0; i < 16; ++i) v[i] *= qs;
      u32x4 p0 = pk8(v, 0), p1 = pk8(v, 1);
      if (padqk) { p1[2] = kone; p1[3] = 0; }
      if (padv) { p0 = (u32x4){vone, vone, vone, vone}; p1 = p0; }
      char* dst = dst0 + (ni * 64 + mi) * tsz;
      st16(dst, p0); st16(dst + 1024, p1);
    }
  ST(2);
}

DI float swapmax(float m) { auto rr = __builtin_amdgcn_permlane32_swap(__float_as_uint(m), __float_as_uint(m), false, false); return fmaxf(__uint_as_float(rr[0]), __uint_as_float(rr[1])); }
DI float max16(const f32x16& s) {
  float a = fmaxf(fmaxf(s[0], s[1]), s[2]), b = fmaxf(fmaxf(s[3], s[4]), s[5]);
  a = fmaxf(fmaxf(a, s[6]), s[7]); b = fmaxf(fmaxf(b, s[8]), s[9]); a = fmaxf(fmaxf(a, s[10]), s[11]); b = fmaxf(fmaxf(b, s[12]), s[13]);
  a = fmaxf(fmaxf(a, s[14]), s[15]); return fmaxf(a, b); }
#define EX2(x) __builtin_amdgcn_exp2f(x)

template <bool CONJ>
DI void cmul2(float a0, float b0, float c0, float s0, float a1, float b1, float c1, float s1, unsigned& re, unsigned& im) {
  float t0, t1, t2, t3;
  if (CONJ)
    asm("v_mul_f32 %2, %7, %9\n\tv_mul_f32 %3, %6, %9\n\tv_mul_f32 %4, %11, %13\n\tv_mul_f32 %5, %10, %13\n\t"
        "v_fma_f32 %2, %6, %8, %2\n\tv_fma_f32 %3, %7, %8, -%3\n\tv_fma_f32 %4, %10, %12, %4\n\tv_fma_f32 %5, %11, %12, -%5\n\t"
        "v_cvt_pk_bf16_f32 %0, %2, %4\n\tv_cvt_pk_bf16_f32 %1, %3, %5"
        : "=v"(re), "=v"(im), "=&v"(t0), "=&v"(t1), "=&v"(t2), "=&v"(t3) : "v"(a0), "v"(b0), "v"(c0), "v"(s0), "v"(a1), "v"(b1), "v"(c1), "v"(s1));
  else
    asm("v_mul_f32 %2, %7, %9\n\tv_mul_f32 %3, %6, %9\n\tv_mul_f32 %4, %11, %13\n\tv_mul_f32 %5, %10, %13\n\t"
        "v_fma_f32 %2, %6, %8, -%2\n\tv_fma_f32 %3, %7, %8, %3\n\tv_fma_f32 %4, %10, %12, -%4\n\tv_fma_f32 %5, %11, %12, %5\n\t"
        "v_cvt_pk_bf16_f32 %0, %2, %4\n\tv_cvt_pk_bf16_f32 %1, %3, %5"
        : "=v"(re), "=v"(im), "=&v"(t0), "=&v"(t1), "=&v"(t2), "=&v"(t3) : "v"(a0), "v"(b0), "v"(c0), "v"(s0), "v"(a1), "v"(b1), "v"(c1), "v"(s1));
}
#define MF_DONE(x, y, z, w) do { __builtin_amdgcn_s_setprio(PRIO_V); asm volatile("s_nop 11" : "+v"(x), "+v"(y), "+v"(z), "+v"(w)); SBAR(); } while (0)
#define VA_DONE(b) do { __builtin_amdgcn_s_setprio(PRIO_M); asm volatile("s_nop 1" : "+v"(b[0]), "+v"(b[1]), "+v"(b[2]), "+v"(b[3]), "+v"(b[4]), "+v"(b[5]), "+v"(b[6]), "+v"(b[7])); SBAR(); } while (0)

DI void a2_column(int c, int lane, const char* __restrict__ ws_in, char* __restrict__ ws, LDS_AS char* tl) {
  const int r = lane & 31, h = lane >> 5, l16 = lane * 16;
  const bool real_col = (c == 0) || (c == 224);
  const int cp = c == 224 ? 1 : 2 * c;
  const rsrc_t rx = mkrsrc(ws_in + WS_X1 + (cp >> 5) * 131072 + (cp & 31) * 16, 0x40000u);
  const rsrc_t rt = mkrsrc(&g_A2, sizeof(g_A2)), rw = mkrsrc(&g_TW, sizeof(g_TW)), rf = mkrsrc(ws_in + WS_WT + c * 8192, 8192);
  const int xv = r * 2048 + h * 512;
#define T32F(mat, t) asb(bld(rt, l16, ((mat) * 2 + (t)) * 1024))
#define T64(mat, m, k) asb(bld(rt, l16, 6144 + (((mat) * 2 + (m)) * 4 + (k)) * 1024))
#define T32I(mat, t) asb(bld(rt, l16, 6144 + 24576 + ((mat) * 2 + (t)) * 1024))
#define TW(m, g, p) __builtin_bit_cast(f32x4, bld(rw, l16, (((m) * 4 + (g)) * 2 + (p)) * 1024))
  bf16x8 ur[2][2], ui[2][2], t32[3][2], t64[3][2][4]; f32x4 tw[2][4][2]; u32x4 wv[2][4];
#pragma unroll
  for (int m = 0; m < 2; ++m)
#pragma unroll
    for (int t = 0; t < 2; ++t) { ur[m][t] = asb(bld(rx, xv, m * 65536 + t * 1024)); ui[m][t] = asb(bld(rx, xv, m * 65536 + t * 1024 + 16)); }
#pragma unroll
  for (int i = 0; i < 6; ++i) t32[i >> 1][i & 1] = T32F(i >> 1, i & 1);
#pragma unroll
  for (int i = 0; i < 16; ++i) tw[i >> 3][(i >> 1) & 3][i & 1] = TW(i >> 3, (i >> 1) & 3, i & 1);
  SBAR();
  if (real_col) {
#pragma unroll
    for (int m = 0; m < 2; ++m)
#pragma unroll
      for (int t = 0; t < 2; ++t) ui[m][t] = (bf16x8){0, 0, 0, 0, 0, 0, 0, 0};
  }
  __builtin_amdgcn_s_setprio(PRIO_M);
  f32x16 re[2], im[2];
#pragma unroll
  for (int m = 0; m < 2; ++m) {
    re[m] = MFMA(ur[m][0], t32[0][0], zero16()); im[m] = MFMA(ui[m][0], t32[0][0], zero16());
    re[m] = MFMA(ur[m][1], t32[0][1], re[m]);    im[m] = MFMA(ui[m][1], t32[0][1], im[m]);
    re[m] = MFMA(ui[m][0], t32[1][0], re[m]);    im[m] = MFMA(ur[m][0], t32[2][0], im[m]);
    re[m] = MFMA(ui[m][1], t32[1][1], re[m]);    im[m] = MFMA(ur[m][1], t32[2][1], im[m]);
  }
  SBAR();
#pragma unroll
  for (int i = 0; i < 12; ++i) t64[i >> 2][0][i & 3] = T64(i >> 2, 0, i & 3);
  MF_DONE(re[0], im[0], re[1], im[1]);
  u32x4 b[8];
#pragma unroll
  for (int m = 0; m < 2; ++m)
#pragma unroll
    for (int e = 0; e < 16; e += 2) { const f32x4 cs = tw[m][e >> 2][0], sn = tw[m][e >> 2][1]; unsigned pr, pi;
      cmul2<true>(re[m][e], im[m][e], cs[e & 3], sn[e & 3], re[m][e + 1], im[m][e + 1], cs[(e & 3) + 1], sn[(e & 3) + 1], pr, pi);
      b[2 * m + (e >> 3)][(e >> 1) & 3] = pr; b[4 + 2 * m + (e >> 3)][(e >> 1) & 3] = pi; }
  SBAR();
#pragma unroll
  for (int i = 0; i < 12; ++i) t64[i >> 2][1][i & 3] = T64(i >> 2, 1, i & 3);
#pragma unroll
  for (int i = 0; i < 8; ++i) wv[i >> 2][i & 3] = bld(rf, l16, i * 1024);
  VA_DONE(b);
#pragma unroll
  for (int m = 0; m < 2; ++m) {
    re[m] = zero16(); im[m] = zero16();
#pragma unroll
    for (int k = 0; k < 4; ++k) {
      re[m] = MFMA(t64[0][m][k], asb(b[k]), re[m]); im[m] = MFMA(t64[0][m][k], asb(b[4 + k]), im[m]);
      re[m] = MFMA(t64[1][m][k], asb(b[4 + k]), re[m]); im[m] = MFMA(t64[2][m][k], asb(b[k]), im[m]);
    }
  }
  SBAR();
  { int l2 = l16; asm volatile("" : "+v"(l2));
#pragma unroll
    for (int i = 0; i < 12; ++i) t64[i >> 2][0][i & 3] = asb(bld(rt, l2, 6144 + ((i >> 2) * 8 + (i & 3)) * 1024)); }
  MF_DONE(re[0], im[0], re[1], im[1]);
  int l3 = l16; asm volatile("" : "+v"(l3));
#pragma unroll
  for (int m = 0; m < 2; ++m)
#pragma unroll
    for (int e = 0; e < 16; e += 2) { const unsigned w0 = wv[m][e >> 2][e & 3], w1 = wv[m][e >> 2][(e & 3) + 1]; unsigned pr, pi;
      cmul2<false>(re[m][e], im[m][e], __uint_as_float(w0 << 16), __uint_as_float(w0 & 0xffff0000u), re[m][e + 1], im[m][e + 1], __uint_as_float(w1 << 16), __uint_as_float(w1 & 0xffff0000u), pr, pi);
      b[2 * m + (e >> 3)][(e >> 1) & 3] = pr; b[4 + 2 * m + (e >> 3)][(e >> 1) & 3] = pi; }
  SBAR();
#pragma unroll
  for (int i = 0; i < 8; ++i) tw[0][i >> 1][i & 1] = __builtin_bit_cast(f32x4, bld(rw, l3, i * 1024));
  VA_DONE(b);
#pragma unroll
  for (int m = 0; m < 2; ++m) {
    re[m] = zero16(); im[m] = zero16();
#pragma unroll
    for (int k = 0; k < 4; ++k) {
      re[m] = MFMA(t64[0][m][k], asb(b[k]), re[m]); im[m] = MFMA(t64[0][m][k], asb(b[4 + k]), im[m]);
      re[m] = MFMA(t64[2][m][k], asb(b[4 + k]), re[m]); im[m] = MFMA(t64[1][m][k], asb(b[k]), im[m]);
    }
    SBAR();
    if (m == 0) {
#pragma unroll
      for (int i = 0; i < 8; ++i) tw[1][i >> 1][i & 1] = __builtin_bit_cast(f32x4, bld(rw, l3, (8 + i) * 1024));
      SBAR();
    }
  }
#pragma unroll
  for (int i = 0; i < 6; ++i) t32[i >> 1][i & 1] = T32I(i >> 1, i & 1);
  MF_DONE(re[0], im[0], re[1], im[1]);
#pragma unroll
  for (int m = 0; m < 2; ++m)
#pragma unroll
    for (int g = 0; g < 4; ++g) { const f32x4 cs = tw[m][g][0], sn = tw[m][g][1]; u32x2 pr, pi; unsigned x, y;
      cmul2<false>(re[m][4 * g], im[m][4 * g], cs[0], sn[0], re[m][4 * g + 1], im[m][4 * g + 1], cs[1], sn[1], x, y); pr[0] = x; pi[0] = y;
      cmul2<false>(re[m][4 * g + 2], im[m][4 * g + 2], cs[2], sn[2], re[m][4 * g + 3], im[m][4 * g + 3], cs[3], sn[3], x, y); pr[1] = x; pi[1] = y;
      LDS_AS char* p = tl + r * 192 + (32 * m + 8 * g + 4 * h) * 2;
      *(LDS_AS u32x2*)p = pr; *(LDS_AS u32x2*)(p + 6144) = pi;
    }
  __builtin_amdgcn_s_setprio(PRIO_M);
  asm volatile("s_waitcnt lgkmcnt(0)" ::: "memory");
  const LDS_AS char* tp = tl + (8 * h + ((lane & 15) >> 2)) * 192 + ((lane >> 4) & 1) * 32 + (lane & 3) * 8;
#pragma unroll
  for (int m = 0; m < 2; ++m) {
    re[m] = zero16(); im[m] = zero16();
#pragma unroll
    for (int t = 0; t < 2; ++t) {
      const bf16x8 ar = ldtr2(tp + t * 16 * 192 + m * 64, 4 * 192), ai = ldtr2(tp + 6144 + t * 16 * 192 + m * 64, 4 * 192);
      re[m] = MFMA(ar, t32[0][t], re[m]); im[m] = MFMA(ai, t32[0][t], im[m]);
      re[m] = MFMA(ai, t32[2][t], re[m]); im[m] = MFMA(ar, t32[1][t], im[m]);
    }
  }
  SBAR(); __builtin_amdgcn_s_setprio(PRIO_V);
  if (!real_col) {
    char* z = ws + WS_Z2 + c * 8192 + lane * 16;
#pragma unroll
    for (int m = 0; m < 2; ++m)
#pragma unroll
      for (int g = 0; g < 4; ++g)
        st16(z + (m * 4 + g) * 1024, (u32x4){pk(re[m][4 * g], im[m][4 * g]), pk(re[m][4 * g + 1], im[m][4 * g + 1]), pk(re[m][4 * g + 2], im[m][4 * g + 2]), pk(re[m][4 * g + 3], im[m][4 * g + 3])});
  } else {
    char* z = ws + WS_Z2 + lane * 16 + (c ? 2 : 0);
#pragma unroll
    for (int m = 0; m < 2; ++m)
#pragma unroll
      for (int g = 0; g < 16; ++g) *(u16*)(z + (m * 4 + (g >> 2)) * 1024 + (g & 3) * 4) = (u16)pk(re[m][g], 0.f);
  }
}

constexpr int K2_EX = 0, K2_MX = 49152, K2_TL = K2_MX + 3072, K2_LDS = K2_TL + 12288;

#define A_MF(d, a, b, c) "v_mfma_f32_32x32x16_bf16 v[" #d ":" #d "+15], v[" #a ":" #a "+3], v[" #b ":" #b "+3], " c "\n\t"
#define A_ACC(d) "v[" #d ":" #d "+15]"
#define A_EXP(s, p, X) X "v_exp_f32 v168, v[" #s "]\n\tv_exp_f32 v169, v[" #s "+1]\n\tv_exp_f32 v170, v[" #s "+2]\n\tv_exp_f32 v171, v[" #s "+3]\n\t" \
                    "v_cvt_pk_bf16_f32 v[" #p "], v168, v169\n\tv_cvt_pk_bf16_f32 v[" #p "+1], v170, v171\n\t"
#define A_PERIOD(SC, SN, KN, QN, OC, V0C, OP, V1P, XA, XB, XC, XD) \
  A_MF(SN, KN, QN, "0")                 A_EXP(SC, 160, XA) \
  A_MF(OP, V1P, 164, A_ACC(OP))         A_EXP(SC + 4, 162, XB) \
  A_MF(SN, KN + 4, QN + 4, A_ACC(SN))   A_EXP(SC + 8, 164, XC) \
  A_MF(OC, V0C, 160, A_ACC(OC))         A_EXP(SC + 12, 166, XD)
#define A_LD(d, so, off) "global_load_dwordx4 v[" #d ":" #d "+3], v172, " so " offset:" #off "\n\t"
#define A_HALF(KC, VC, KO, VO, PC, PC0, PC1, PO)   \
  A_PERIOD(64, 80, KC, 104, 0, VC, 48, VO + 4, "", A_LD(VO, PO, 2048) A_LD(VO + 4, PO, 3072), "s_waitcnt vmcnt(4)\n\t", "") \
  A_PERIOD(80, 64, KC, 112, 16, VC, 0, VC + 4, "s_add_u32 %[off], %[off], 0x1000\n\ts_and_b32 %[off], %[off], 0x3ffff\n\t", "s_add_u32 " PC0 ", %[kvlo], %[off]\n\ts_addc_u32 " PC1 ", %[kvhi], 0\n\t", "", "") \
  A_PERIOD(64, 80, KC, 120, 32, VC, 16, VC + 4, "", "", "", "s_waitcnt vmcnt(2)\n\t") \
  A_PERIOD(80, 64, KO, 96, 48, VC, 32, VC + 4, A_LD(KC, PC, 0) A_LD(KC + 4, PC, 1024), "", "", "s_add_u32 %[cnt], %[cnt], 1\n\t")

__global__ __launch_bounds__(320, 2) void k2_attn(const char* __restrict__ wsi, char* __restrict__ ws) {
  __shared__ __attribute__((aligned(1024))) char smem[K2_LDS];
  LDS_AS char* lds = (LDS_AS char*)smem;
  const int bid = blockIdx.x, xcd = bid & 7, jj = bid >> 3;
  const int head = 2 * xcd + (jj & 1), qb = jj >> 1;
  const int lane = threadIdx.x & 63, wave = __builtin_amdgcn_readfirstlane(threadIdx.x >> 6);
  const int h = lane >> 5;
  const int col = (jj >> 2) * 32 + xcd * 4 + (jj & 3);
  STAMP_DECL;
  if (wave == 4) {
    __builtin_amdgcn_s_sleep(A2_SLEEP);
    ST(0);
    if (col < DF) a2_column(col, lane, wsi, ws, lds + K2_TL);
    ST(1); STAMP_FLUSH(ws, 4);
    return;
  }
  ST(0);
  const int n0 = col < DF ? K2_N0 : 16, no = (64 - n0 + 2) / 3;
  const int st = wave == 0 ? 0 : n0 + (wave - 1) * no, nt = wave == 0 ? n0 : (wave == 3 ? 64 - st : no);
  const rsrc_t rkv = mkrsrc(wsi + WS_KV + head * 262144, 262144), rq = mkrsrc(wsi + WS_QF + (head * 64 + 4 * qb) * 2048, 8192);
  int offA = ((st + 4 * qb) & 63) << 12, offB = (offA + 4096) & 0x3ffff;
  const int l16 = lane * 16;
  u32x4 KA0 = bld(rkv, l16, offA), KA1 = bld(rkv, l16 + 1024, offA);
  u32x4 Q0[4], Q1[4];
#pragma unroll
  for (int t = 0; t < 4; ++t) { const int o = ((wave + t) & 3) * 2048; Q0[t] = bld(rq, l16, o); Q1[t] = bld(rq, l16 + 1024, o); }
  u32x4 VA0 = bld(rkv, l16 + 2048, offA), VA1 = bld(rkv, l16 + 3072, offA), KB0 = bld(rkv, l16, offB), KB1 = bld(rkv, l16 + 1024, offB);
  f32x16 O[4]; float mref[4];
#pragma unroll
  for (int t = 0; t < 4; ++t) {
    f32x16 s = MFMA(asb(KA0), asb(Q0[t]), zero16()); s = MFMA(asb(KA1), asb(Q1[t]), s);
    const float m = swapmax(max16(s));
    const unsigned nb = pk(-m, 0.f);
    mref[t] = -__uint_as_float(nb << 16);
    Q1[t][2] = h == 0 ? nb : Q1[t][2];
    O[t] = zero16();
  }
  ST(1);
  unsigned long long cy0 = CYC();
  u32x4 VB0, VB1 = {0, 0, 0, 0};
  {
    f32x16 SA = MFMA(asb(KA0), asb(Q0[0]), zero16()); SA = MFMA(asb(KA1), asb(Q1[0]), SA);
    int cnt = -nt;
    const uint64_t kvs = (uint64_t)(wsi + WS_KV + head * 262144);
    u32x4 P1 = {0, 0, 0, 0};
    asm volatile(
      "s_add_u32 s42, %[kvlo], %[off]\n\ts_addc_u32 s43, %[kvhi], 0\n\ts_nop 9\n"
      "Ltop%=:\n\t"
      A_HALF(128, 136, 144, 152, "s[40:41]", "s40", "s41", "s[42:43]")
      "s_cbranch_scc1 LendA%=\n\t"
      A_HALF(144, 152, 128, 136, "s[42:43]", "s42", "s43", "s[40:41]")
      "s_cbranch_scc0 Ltop%=\n\t"
      "s_nop 0\n\t"
      A_MF(48, 156, 164, A_ACC(48))
      "s_branch Ldone%=\n"
      "LendA%=:\n\t"
      "s_nop 0\n\t"
      A_MF(48, 140, 164, A_ACC(48))
      "Ldone%=:\n\t"
      "s_nop 11"
      : "+{v[0:15]}"(O[0]), "+{v[16:31]}"(O[1]), "+{v[32:47]}"(O[2]), "+{v[48:63]}"(O[3]), "+{v[64:79]}"(SA),
        "+{v[128:131]}"(KA0), "+{v[132:135]}"(KA1), "+{v[136:139]}"(VA0), "+{v[140:143]}"(VA1), "+{v[144:147]}"(KB0), "+{v[148:151]}"(KB1),
        "={v[152:155]}"(VB0), "+{v[156:159]}"(VB1), "+{v[164:167]}"(P1), [off] "+s"(offB), [cnt] "+s"(cnt)
      : "{v[96:99]}"(Q0[0]), "{v[100:103]}"(Q1[0]), "{v[104:107]}"(Q0[1]), "{v[108:111]}"(Q1[1]), "{v[112:115]}"(Q0[2]), "{v[116:119]}"(Q1[2]), "{v[120:123]}"(Q0[3]), "{v[124:127]}"(Q1[3]),
        "{v172}"(l16), [kvlo] "s"((unsigned)kvs), [kvhi] "s"((unsigned)(kvs >> 32))
      : "memory", "scc", "s40", "s41", "s42", "s43", "v80", "v81", "v82", "v83", "v84", "v85", "v86", "v87", "v88", "v89", "v90", "v91", "v92", "v93", "v94", "v95",
        "v160", "v161", "v162", "v163", "v168", "v169", "v170", "v171");
  }
  SC(5, cy0);
  ST(2);
#pragma unroll
  for (int t = 1; t < 4; ++t) {
    const int e = ((wave + t) & 3) * 3 + t - 1;
    LDS_AS char* p = lds + K2_EX + e * 4096 + l16;
#pragma unroll
    for (int g = 0; g < 4; ++g) *(LDS_AS f32x4*)(p + g * 1024) = (f32x4){O[t][4 * g], O[t][4 * g + 1], O[t][4 * g + 2], O[t][4 * g + 3]};
    *(LDS_AS float*)(lds + K2_MX + e * 256 + lane * 4) = mref[t];
  }
  asm volatile("s_waitcnt vmcnt(0) lgkmcnt(0)\n\ts_barrier"
               : "+{v[128:131]}"(KA0), "+{v[132:135]}"(KA1), "+{v[136:139]}"(VA0), "+{v[140:143]}"(VA1), "+{v[144:147]}"(KB0), "+{v[148:151]}"(KB1), "+{v[152:155]}"(VB0), "+{v[156:159]}"(VB1) :: "memory");
  ST(3);
  {
    f32x16 acc = O[0];
    float mw[3]; float M = mref[0];
#pragma unroll
    for (int k = 0; k < 3; ++k) { mw[k] = *(const LDS_AS float*)(lds + K2_MX + (wave * 3 + k) * 256 + lane * 4); M = fmaxf(M, mw[k]); }
    float lmin = acc[12], lmax = acc[12];
    const float fo = EX2(mref[0] - M);
#pragma unroll
    for (int i = 0; i < 16; ++i) acc[i] *= fo;
#pragma unroll
    for (int k = 0; k < 3; ++k) { const float f = EX2(mw[k] - M);
      const LDS_AS char* e = lds + K2_EX + (wave * 3 + k) * 4096 + l16;
#pragma unroll
      for (int g = 0; g < 4; ++g) { const f32x4 v = *(const LDS_AS f32x4*)(e + g * 1024);
        if (g == 3) { lmin = fminf(lmin, v[0]); lmax = fmaxf(lmax, v[0]); }
#pragma unroll
        for (int i = 0; i < 4; ++i) acc[4 * g + i] = fmaf(v[i], f, acc[4 * g + i]); } }
    float sm = 0.f;
#pragma unroll
    for (int i = 0; i < 16; ++i) sm += acc[i];
    const bool ok = (h == 1 || (lmin >= 0x1p-40f && lmax <= 0x1p40f)) && fabsf(sm) <= 3.0e38f;
    if (__builtin_expect(!__all(ok), 0)) {
      const bf16x8 q0 = asb(bld(rq, l16, wave * 2048)), q1 = asb(bld(rq, l16 + 1024, wave * 2048));
      float m = -INFINITY; acc = zero16();
      for (int kt = 0; kt < 64; ++kt) {
        f32x16 s = MFMA(asb(bld(rkv, l16, kt << 12)), q0, zero16()); s = MFMA(asb(bld(rkv, l16 + 1024, kt << 12)), q1, s);
        const float mn = fmaxf(m, swapmax(max16(s)));
        const float al = EX2(m - mn); m = mn;
#pragma unroll
        for (int i = 0; i < 16; ++i) { acc[i] *= al; s[i] = EX2(s[i] - mn); }
        acc = MFMA(asb(bld(rkv, l16 + 2048, kt << 12)), asb(pk8(s, 0)), acc); acc = MFMA(asb(bld(rkv, l16 + 3072, kt << 12)), asb(pk8(s, 1)), acc);
      }
    }
    auto rr = __builtin_amdgcn_permlane32_swap(__float_as_uint(acc[12]), __float_as_uint(acc[12]), false, false);
    const float inv = __builtin_amdgcn_rcpf(__uint_as_float(rr[0]));
#pragma unroll
    for (int i = 0; i < 16; ++i) acc[i] *= inv;
    u32x4 p0 = pk8(acc, 0), p1 = pk8(acc, 1);
    if (h == 0) { p1[2] = 0; p1[3] = 0; }
    char* dst = ws + WS_OF + ((4 * qb + wave) * 16 + head) * 2048 + l16;
    st16(dst, p0); st16(dst + 1024, p1);
  }
  ST(4);
  if (wave == 0) STAMP_FLUSH(ws, 1);
  if (wave == 1) STAMP_FLUSH(ws, 5);
}

__global__ __launch_bounds__(512, 2) void k3_out(const char* __restrict__ ws, const u16* __restrict__ wo, u16* __restrict__ out) {
  __shared__ __attribute__((aligned(1024))) char smem[57344];
  LDS_AS char* lds = (LDS_AS char*)smem;
  const int bid = blockIdx.x, xcd = bid & 7, jj = bid >> 3;
  const int part = xcd & 1, mt = 4 * (xcd >> 1) + (jj & 3), nt = jj >> 2;
  const int lane = threadIdx.x & 63, wave = __builtin_amdgcn_readfirstlane(threadIdx.x >> 6);
  if (wave >= 4) {
    const int lw = wave - 4, rr = lane >> 3, sl = lane & 7;
    const char* src; int pstride;
    if (part) { src = (const char*)wo + nt * 128 + rr * 896 + (((sl >> 2) ^ ((rr >> 1) & 1)) * 64) + (sl & 3) * 16 + lw * 8 * 896; pstride = 32 * 896; }
    else { src = nullptr; pstride = 0; }
#pragma unroll
    for (int i = 0; i < 14; ++i) {
      const int p = lw + 4 * i;
      const char* g;
      if (part) g = src + i * pstride;
      else { const int seg = p >> 3, row = 8 * (p & 7) + rr; g = (const char*)&g_F + (64 * nt + row) * 896 + seg * 128 + ((sl ^ ((row >> 1) & 7)) * 16); }
      dma16(g, lds + p * 1024);
    }
    asm volatile("s_waitcnt vmcnt(10)\n\ts_barrier" ::: "memory");
    asm volatile("s_waitcnt vmcnt(6)\n\ts_barrier" ::: "memory");
    asm volatile("s_waitcnt vmcnt(2)\n\ts_barrier" ::: "memory");
    asm volatile("s_waitcnt vmcnt(0)\n\ts_barrier" ::: "memory");
    return;
  }
  const int r = lane & 31, h = lane >> 5, tt = 4 * mt + wave;
  STAMP_DECL; ST(0);
  f32x16 acc0 = zero16(), acc1 = zero16();
  int tok, colb;
  if (part) {
    bf16x8 tk[32];
    const char* src = ws + WS_OF + tt * 32768 + lane * 16;
#pragma unroll
    for (int s = 0; s < 32; ++s) tk[s] = *(const bf16x8*)(src + s * 1024);
    const int q = (lane & 15) >> 2;
    const LDS_AS char* w0 = lds + (4 * h + q) * 128 + ((q >> 1) * 64) + ((lane >> 4) & 1) * 32 + (lane & 3) * 8;
    const LDS_AS char* w1 = lds + (4 * h + q) * 128 + (((q >> 1) ^ 1) * 64) + ((lane >> 4) & 1) * 32 + (lane & 3) * 8;
#pragma unroll
    for (int s = 0; s < 32; ++s) {
      if ((s & 7) == 0) asm volatile("s_barrier" ::: "memory");
      const int o = (s >> 1) * 3584 + (s & 1) * 2048, o2 = (s & 1) ? 512 : 1024;
      acc0 = MFMA(ldtr2(w0 + o, o2), tk[s], acc0); acc1 = MFMA(ldtr2(w1 + o, o2), tk[s], acc1);
    }
    tok = 32 * tt + r; colb = 448 + 64 * nt;
  } else {
    u32x4 tk[28];
    const char* src = ws + WS_Z2 + h * 4 * 8192 + (32 * tt + r) * 4;
#pragma unroll
    for (int s = 0; s < 28; ++s)
#pragma unroll
      for (int i = 0; i < 4; ++i) tk[s][i] = *(const unsigned*)(src + (8 * s + i) * 8192);
    const int sw = (r >> 1) & 7;
    const LDS_AS char* wa = lds + r * 128;
#pragma unroll
    for (int s = 0; s < 28; ++s) {
      if ((s & 7) == 0) asm volatile("s_barrier" ::: "memory");
      const LDS_AS char* p = wa + (s >> 2) * 8192 + (((2 * (s & 3) + h) ^ sw) * 16);
      acc0 = MFMA(ldsr(p), asb(tk[s]), acc0); acc1 = MFMA(ldsr(p + 4096), asb(tk[s]), acc1);
    }
    const int p = 32 * tt + r, l2 = (p >> 2) & 63, mg = p >> 8;
    tok = 64 * (l2 & 31) + 32 * (mg >> 2) + 8 * (mg & 3) + 4 * (l2 >> 5) + (p & 3); colb = 64 * nt;
  }
  ST(1);
  char* dst = (char*)out + tok * 1792 + colb * 2 + h * 16;
#pragma unroll
  for (int nb = 0; nb < 2; ++nb) {
    const f32x16& a = nb ? acc1 : acc0;
#pragma unroll
    for (int pr = 0; pr < 2; ++pr) {
      const unsigned a0 = pk(a[8 * pr], a[8 * pr + 1]), a1 = pk(a[8 * pr + 2], a[8 * pr + 3]), b0 = pk(a[8 * pr + 4], a[8 * pr + 5]), b1 = pk(a[8 * pr + 6], a[8 * pr + 7]);
      auto x = __builtin_amdgcn_permlane32_swap(a0, b0, false, false);
      auto y = __builtin_amdgcn_permlane32_swap(a1, b1, false, false);
      st16o(dst + nb * 64 + pr * 32, (u32x4){x[0], y[0], x[1], y[1]});
    }
  }
  ST(2);
  if (wave == 0) STAMP_FLUSH(ws, 2);
}

__global__ void k_first(char* ws) { if (threadIdx.x == 0) *(unsigned long long*)(ws + (32u << 20)) = __builtin_amdgcn_s_memrealtime(); }
__global__ void k_last(const char* ws) { { const unsigned long long t0 = *(const volatile unsigned long long*)(ws + (32u << 20)); for (int i = 0; i < 200000; ++i) { if (__builtin_amdgcn_s_memrealtime() - t0 >= 4500ull) break; __builtin_amdgcn_s_sleep(1); } } }
extern "C" void kernel_launch(void* const* d_in, const int* in_sizes, int n_in, void* d_out, int out_size, void* d_ws, size_t ws_size, hipStream_t stream) {
  char* ws = (char*)d_ws;
  k_first<<<1, 64, 0, stream>>>(ws);
  k1_proj<<<256, 512, 0, stream>>>((const u16*)d_in[0], (const u16*)d_in[1], (const u16*)d_in[2], (const u16*)d_in[3], (const u16*)d_in[4], ws);
  k2_attn<<<256, 320, 0, stream>>>(ws, ws);
  k3_out<<<224, 512, 0, stream>>>(ws, (const u16*)d_in[5], (u16*)d_out);
  k_last<<<1, 64, 0, stream>>>(ws);
}
```

```cpp
#include <hip/hip_runtime.h>
#include <stdint.h>

typedef unsigned short u16;
typedef short bf16x8 __attribute__((ext_vector_type(8)));
typedef short s16x4 __attribute__((ext_vector_type(4)));
typedef float f32x16 __attribute__((ext_vector_type(16)));
typedef float f32x4 __attribute__((ext_vector_type(4)));
typedef float f32x2 __attribute__((ext_vector_type(2)));
typedef unsigned u32x4 __attribute__((ext_vector_type(4)));
typedef unsigned u32x2 __attribute__((ext_vector_type(2)));
typedef __bf16 bf16x2_t __attribute__((ext_vector_type(2)));
#define LDS_AS __attribute__((address_space(3)))
#define DI __device__ __forceinline__
#define MFMA(a, b, c) __builtin_amdgcn_mfma_f32_32x32x16_bf16((a), (b), (c), 0, 0, 0)
#define SBAR() __builtin_amdgcn_sched_barrier(0)
#define PIN(x) asm volatile("" : "+v"(x))

constexpr int DF = 225;

namespace ct {
constexpr double PI = 3.14159265358979323846264338327950288;
constexpr double tsin(double x) { double x2 = x * x, t = x, s = x; for (int i = 1; i < 12; ++i) { t *= -x2 / ((2 * i) * (2 * i + 1)); s += t; } return s; }
constexpr double tcos(double x) { double x2 = x * x, t = 1, s = 1; for (int i = 1; i < 12; ++i) { t *= -x2 / ((2 * i - 1) * (2 * i)); s += t; } return s; }
constexpr double cos2pi(int j, int N) {
  j %= N; if (j < 0) j += N; if (2 * j > N) j = N - j; bool neg = false; if (4 * j > N) { j = N / 2 - j; neg = true; }
  double r = (8 * j > N) ? tsin(2 * PI * (N / 4 - j) / N) : tcos(2 * PI * j / N); return neg ? -r : r; }
constexpr double sin2pi(int j, int N) { return cos2pi(j - N / 4, N); }
constexpr u16 bf(double v) { float f = (float)v; unsigned u = __builtin_bit_cast(unsigned, f); u += 0x7fffu + ((u >> 16) & 1u); return (u16)(u >> 16); }
struct C448 { u16 c[448]; u16 ns[448]; };
constexpr C448 mk448() { C448 t{}; for (int j = 0; j < 448; ++j) { t.c[j] = bf(cos2pi(j, 448)); t.ns[j] = bf(-sin2pi(j, 448)); } return t; }
constexpr C448 T448 = mk448();
struct FPart { u16 v[56 * 448]; };
constexpr FPart mkF(int part) { FPart t{};
  for (int r = 0; r < 56; ++r) { int d = part * 56 + r; int a = 0; u16* o = &t.v[r * 448];
    for (int c = 0; c < 224; ++c) { o[2 * c] = T448.c[a]; o[2 * c + 1] = T448.ns[a]; a += d; if (a >= 448) a -= 448; }
    o[1] = (d & 1) ? 0xbf80 : 0x3f80; }
  return t; }
constexpr FPart F0 = mkF(0), F1 = mkF(1), F2 = mkF(2), F3 = mkF(3), F4 = mkF(4), F5 = mkF(5), F6 = mkF(6), F7 = mkF(7);
struct FTab { FPart p0, p1, p2, p3, p4, p5, p6, p7; u16 pad[128]; };
constexpr int kperm(int t, int h, int j) { return 16 * t + 8 * (j >> 2) + 4 * h + (j & 3); }
struct A2Tab {
  u16 t32f[3][2][64][8];
  u16 t64[3][2][4][64][8];
  u16 t32i[3][2][64][8];
};
constexpr A2Tab mkA2() { A2Tab t{};
  const double SC = 2.0 / (2048.0 * 448.0);
  for (int s = 0; s < 2; ++s) for (int l = 0; l < 64; ++l) for (int j = 0; j < 8; ++j) {
    int a = kperm(s, l >> 5, j) * (l & 31);
    t.t32f[0][s][l][j] = bf(cos2pi(a, 32)); t.t32f[1][s][l][j] = bf(sin2pi(a, 32)); t.t32f[2][s][l][j] = bf(-sin2pi(a, 32));
    int b = (16 * s + 8 * (l >> 5) + j) * (l & 31);
    t.t32i[0][s][l][j] = bf(SC * cos2pi(b, 32)); t.t32i[1][s][l][j] = bf(SC * sin2pi(b, 32)); t.t32i[2][s][l][j] = bf(-SC * sin2pi(b, 32)); }
  for (int m = 0; m < 2; ++m) for (int k = 0; k < 4; ++k) for (int l = 0; l < 64; ++l) for (int j = 0; j < 8; ++j) {
    int a = (32 * m + (l & 31)) * (32 * (k >> 1) + kperm(k & 1, l >> 5, j));
    t.t64[0][m][k][l][j] = bf(cos2pi(a, 64)); t.t64[1][m][k][l][j] = bf(sin2pi(a, 64)); t.t64[2][m][k][l][j] = bf(-sin2pi(a, 64)); }
  return t; }
struct TwTab { float tw[2][4][2][64][4]; };
constexpr TwTab mkTw() { TwTab t{};
  for (int m = 0; m < 2; ++m) for (int g = 0; g < 4; ++g) for (int l = 0; l < 64; ++l) for (int i = 0; i < 4; ++i) {
    int a = (32 * m + 8 * g + 4 * (l >> 5) + i) * (l & 31);
    t.tw[m][g][0][l][i] = (float)cos2pi(a, 2048); t.tw[m][g][1][l][i] = (float)sin2pi(a, 2048); }
  return t; }
}
__device__ __attribute__((aligned(256))) const ct::FTab g_F = { ct::F0, ct::F1, ct::F2, ct::F3, ct::F4, ct::F5, ct::F6, ct::F7, {} };
__device__ __attribute__((aligned(256))) const ct::A2Tab g_A2 = ct::mkA2();
__device__ __attribute__((aligned(256))) const ct::TwTab g_TW = ct::mkTw();

constexpr int QH = 131072 + 8192, KH = 262144 + 16384, OQ = 32768 + 4096;
constexpr size_t WS_QF = 0;
constexpr size_t WS_KV = 3u << 20;
constexpr size_t WS_X1 = 8u << 20;
constexpr size_t WS_WT = 10u << 20;
constexpr size_t WS_OF = 12u << 20;
constexpr size_t WS_Z2 = 15u << 20;
constexpr size_t WS_SY = WS_Z2 + (2u << 20);
constexpr int SY_P1 = 16, SY_PW = SY_P1 + 256, SY_PO = SY_PW + 256, SY_PZ = SY_PO + 256;
__device__ __forceinline__ int sy_off(int i) { return (i >> 4) * 4352 + (i & 15) * 8; }

DI unsigned pk(float a, float b) { f32x2 v = {a, b}; return __builtin_bit_cast(unsigned, __builtin_convertvector(v, bf16x2_t)); }
DI u32x4 pk8(const f32x16& x, int s) { u32x4 r; r[0] = pk(x[8 * s], x[8 * s + 1]); r[1] = pk(x[8 * s + 2], x[8 * s + 3]); r[2] = pk(x[8 * s + 4], x[8 * s + 5]); r[3] = pk(x[8 * s + 6], x[8 * s + 7]); return r; }
DI bf16x8 asb(u32x4 v) { return __builtin_bit_cast(bf16x8, v); }
DI bf16x8 ldsr(const LDS_AS char* p) { return *(const LDS_AS bf16x8*)p; }
DI s16x4 ldtr(const LDS_AS char* p) { return __builtin_amdgcn_ds_read_tr16_b64_v4i16((LDS_AS s16x4*)p); }
DI bf16x8 ldtr2(const LDS_AS char* p, int off2) { s16x4 lo = ldtr(p), hi = ldtr(p + off2); return __builtin_shufflevector(lo, hi, 0, 1, 2, 3, 4, 5, 6, 7); }
DI void dma16(const char* g, LDS_AS char* l) { __builtin_amdgcn_global_load_lds((const __attribute__((address_space(1))) unsigned*)g, (LDS_AS unsigned*)l, 16, 0, 0); }
DI void touch(const void* p, unsigned lds_addr) { unsigned sv;
  asm volatile("s_mov_b32 %0, m0\n\ts_mov_b32 m0, %2\n\ts_nop 0\n\tglobal_load_lds_dword %1, off\n\ts_mov_b32 m0, %0" : "=&s"(sv) : "v"(p), "s"(lds_addr)); }
#define WAITV(n) asm volatile("s_waitcnt vmcnt(" #n ")" ::: "memory")
#define BARRIER() asm volatile("s_waitcnt lgkmcnt(0)\n\ts_barrier" ::: "memory")
DI const f32x16 zero16() { f32x16 z; for (int i = 0; i < 16; ++i) z[i] = 0.f; return z; }

typedef __amdgpu_buffer_rsrc_t rsrc_t;
typedef __attribute__((address_space(1))) unsigned long long gu64;
typedef unsigned long long u64;
constexpr int LD_AUX = 16;
DI rsrc_t mkrsrc(const void* p, unsigned bytes) { return __builtin_amdgcn_make_buffer_rsrc((void*)p, 0, bytes, 0x00020000); }
DI u32x4 bld(rsrc_t r, int voff, int soff) { return __builtin_amdgcn_raw_buffer_load_b128(r, voff, soff, 0); }
DI u32x4 bldh(rsrc_t r, int voff, int soff) { return __builtin_amdgcn_raw_buffer_load_b128(r, voff, soff, LD_AUX); }
DI void bsth(u32x4 v, rsrc_t r, int voff, int soff) { __builtin_amdgcn_raw_buffer_store_b128(v, r, voff + soff, 0, 16); }
#define RLX_AGENT __ATOMIC_RELAXED, __HIP_MEMORY_SCOPE_AGENT
DI void flag_set(char* ws, int off, u64 tag, int lane) { if (lane == 0) __hip_atomic_store((gu64*)(ws + WS_SY + sy_off(off)), tag, RLX_AGENT); }
template <int N>
DI void flag_wait(const char* ws, const int (&off)[N], u64 tag) {
#pragma unroll 1
  for (int spin = 0; spin < (1 << 18); ++spin) {
    bool ok = true;
#pragma unroll
    for (int i = 0; i < N; ++i) ok &= __hip_atomic_load((gu64*)(ws + WS_SY + sy_off(off[i])), RLX_AGENT) == tag;
    if (__all(ok)) break;
    __builtin_amdgcn_s_sleep(3);
  }
  __builtin_amdgcn_fence(__ATOMIC_ACQUIRE, "wavefront");
}
#define DRAINBAR() asm volatile("s_waitcnt vmcnt(0) lgkmcnt(0)\n\ts_barrier" ::: "memory")

template <int N> struct IntC { static constexpr int value = N; };
template <bool TRANS, int WROW>
DI void k1_loop(const LDS_AS char* lds, int lane, int wm, int wn, f32x16 (&acc)[2][2]) {
  constexpr int SLOT = 16384 + 64 * WROW;
  const int r = lane & 31, h = lane >> 5;
  const int sw = (r >> 1) & 7;
  const LDS_AS char* xa[4];
#pragma unroll
  for (int s = 0; s < 4; ++s) xa[s] = lds + (wm * 64 + r) * 128 + ((2 * s + h) ^ sw) * 16;
  const int q = (lane & 15) >> 2, c4 = ((lane >> 4) & 1) * 4 + (lane & 3);
  const LDS_AS char* w0 = lds + 16384 + (WROW == 256 ? (8 * h + q) * 256 + (((2 * wn) ^ q) * 64) + c4 * 8 : (8 * h + 2 * q) * 224 + 2 * wn * 56 + (c4 >= 6 ? c4 - 1 : c4) * 8);
  const LDS_AS char* w1 = WROW == 256 ? lds + 16384 + (8 * h + q) * 256 + (((2 * wn + 1) ^ q) * 64) + c4 * 8 : w0 + 56;
  constexpr int T1 = WROW == 256 ? 1024 : 224;
  bf16x8 fa0[3], fa1[3], fb0[3], fb1[3];
#define K1_XO(j) ((((j) >> 2) & 3) * SLOT)
#define K1_WO(j) ((((j) >> 2) & 3) * SLOT + ((j) & 3) * 16 * WROW)
#define K1_LA(j) do { fa0[(j) % 3] = ldsr(xa[(j) & 3] + K1_XO(j)); fb0[(j) % 3] = ldtr2(w0 + K1_WO(j), T1); } while (0)
#define K1_LB(j) do { fb1[(j) % 3] = ldtr2(w1 + K1_WO(j), T1); fa1[(j) % 3] = ldsr(xa[(j) & 3] + K1_XO(j) + 4096); } while (0)
#define K1_MF(i, jj, A, B) acc[i][jj] = TRANS ? MFMA(B, A, j ? acc[i][jj] : zero16()) : MFMA(A, B, j ? acc[i][jj] : zero16())
  BARRIER();
  K1_LA(0); K1_LB(0); K1_LA(1); K1_LB(1);
#pragma unroll
  for (int j = 0; j < 28; ++j) {
    SBAR();
    K1_MF(0, 0, fa0[j % 3], fb0[j % 3]); SBAR();
    if ((j & 3) == 2 && j < 24) BARRIER();
    if (j + 2 < 28) K1_LA(j + 2);
    SBAR();
    K1_MF(0, 1, fa0[j % 3], fb1[j % 3]); SBAR();
    if (j + 2 < 28) K1_LB(j + 2);
    SBAR();
    K1_MF(1, 0, fa1[j % 3], fb0[j % 3]); SBAR();
    K1_MF(1, 1, fa1[j % 3], fb1[j % 3]);
  }
  SBAR();
}

template <class H1>
DI void k1_loader(LDS_AS char* lds, int lane, int lw, int mt, int nt, const u16* ax, const u16* bx, const u16* wqkv, H1&& h1) {
  const bool spec = nt >= 12, four = spec || lw < 2;
  const char* xb = (const char*)(spec ? ax : bx);
  const char* wbase = spec ? (const char*)&g_F + (nt - 12) * 256 : (const char*)wqkv + nt * 224;
  const int ldb64 = spec ? 64 * 896 : 64 * 2688, slot = spec ? 32768 : 30720;
  const int rot = (2 * (nt & 3) + (mt & 7)) % 7;
  const int wp0 = four ? 4 * lw : 3 * lw + 2;
  unsigned vx[4], vw[4];
  const unsigned vx0 = (spec ? (64 * (lane >> 3) + 4 * mt + lw) : (128 * mt + 32 * lw + (lane >> 3))) * 896, vxs = spec ? 512 * 896 : 8 * 896;
#pragma unroll
  for (int i = 0; i < 4; ++i) vx[i] = vx0 + i * vxs + (((lane & 7) ^ ((4 * i + (lane >> 4)) & 7)) * 16);
  const unsigned l0 = (unsigned)(uintptr_t)lds;
#define K1_DMA(voff, base, dst) asm volatile("s_mov_b32 m0, %2\n\ts_nop 0\n\tglobal_load_lds_dwordx4 %0, %1" :: "v"(voff), "s"(base), "s"(dst) : "memory")
  auto issue_x = [&](int c) {
    const int cs = c + rot >= 7 ? c + rot - 7 : c + rot;
    const char* g = xb + cs * 128; const unsigned l = l0 + (c & 3) * slot + lw * 4096;
#pragma unroll
    for (int i = 0; i < 4; ++i) K1_DMA(vx[i], g, l + i * 1024);
  };
  auto issue_w = [&](int c) {
    const int cs = c + rot >= 7 ? c + rot - 7 : c + rot;
    const char* g = wbase + cs * ldb64; const unsigned l = l0 + (c & 3) * slot + 16384 + wp0 * 1024;
#pragma unroll
    for (int i = 0; i < 3; ++i) K1_DMA(vw[i], g, l + i * 1024);
    if (four) K1_DMA(vw[3], g, l + 3072);
  };
  auto wb = [&](auto N8, auto N7) {
    if (four) asm volatile("s_waitcnt vmcnt(%0)" :: "i"(N8.value) : "memory"); else asm volatile("s_waitcnt vmcnt(%0)" :: "i"(N7.value) : "memory");
    asm volatile("s_barrier" ::: "memory"); };
  issue_x(0);
#pragma unroll
  for (int i = 0; i < 4; ++i) {
    const int wrow = 4 * (wp0 + i) + (lane >> 4);
    const int idx = 64 * (wp0 + i) + lane, pos = (idx * 4682) >> 16, pc = idx - 14 * pos;
    const int k = (pos & ~7) | ((pos & 1) << 2) | ((pos >> 1) & 3);
    vw[i] = spec ? wrow * 896 + (((lane >> 2) & 3) ^ (wrow & 3)) * 64 + (lane & 3) * 16 : k * 2688 + pc * 16;
  }
  issue_w(0); issue_x(1); issue_w(1); issue_x(2); issue_w(2);
#pragma unroll 1
  for (int c = 3; c < 7; ++c) { wb(IntC<16>{}, IntC<14>{}); issue_x(c); issue_w(c); }
  h1();
  wb(IntC<19>{}, IntC<17>{}); wb(IntC<11>{}, IntC<10>{}); wb(IntC<3>{}, IntC<3>{});
}

DI int filt_rowbyte(int bid, int row) { return (128 * (bid >> 4) + 32 * (row >> 3) + 8 * ((bid >> 2) & 3) + (row & 7)) * (DF * 2) + 114 * (bid & 3); }
DI void filt_dma(int lw, int lane, int bid, const u16* wr, const u16* wi, unsigned scr) {
#pragma unroll
  for (int i = 0; i < 3; ++i) {
    int t = (lw * 3 + i) * 64 + lane; t = t > 575 ? 575 : t;
    const int ar = (t * 7282) >> 16, ch = t - 9 * ar;
    int off = (filt_rowbyte(bid, ar & 31) & ~15) + 16 * ch; off = off > 2048 * DF * 2 - 16 ? 2048 * DF * 2 - 16 : off;
    const char* g = (const char*)(ar >= 32 ? wi : wr) + off;
    asm volatile("s_mov_b32 m0, %1\n\ts_nop 0\n\tglobal_load_lds_dwordx4 %0, off" :: "v"(g), "s"(scr + (lw * 3 + i) * 1024) : "memory");
  }
}
DI void filt_gather(int tid, int bid, const LDS_AS char* scr, u32x4 (&q)[2], int (&off)[2]) {
  const int a = bid >> 4, f1l = tid & 7;
#pragma unroll
  for (int k = 0; k < 2; ++k) {
    int cl = (tid >> 3) + 32 * k; cl = cl > 56 ? 56 : cl; cl = 57 * (bid & 3) + cl > 224 ? 224 - 57 * (bid & 3) : cl;
    const int c = 57 * (bid & 3) + cl;
    const bool half = (c == 0) || (c == 224);
#pragma unroll
    for (int i = 0; i < 4; ++i) {
      const LDS_AS char* p = scr + (8 * i + f1l) * 144 + (filt_rowbyte(bid, 8 * i + f1l) & 15) + 2 * cl;
      unsigned re = (unsigned)*(const LDS_AS u16*)p << 16, im = (unsigned)*(const LDS_AS u16*)(p + 32 * 144) << 16;
      if (half) { re = __float_as_uint(__uint_as_float(re) * 0.5f); im = __float_as_uint(__uint_as_float(im) * 0.5f); }
      q[k][i] = (re >> 16) | (im & 0xffff0000u);
    }
    off[k] = c * 8192 + ((a >> 1) * 64 + 8 * ((bid >> 2) & 3) + f1l + 32 * (a & 1)) * 16;
  }
}

DI float swapmax(float m) { auto rr = __builtin_amdgcn_permlane32_swap(__float_as_uint(m), __float_as_uint(m), false, false); return fmaxf(__uint_as_float(rr[0]), __uint_as_float(rr[1])); }
DI float max16(const f32x16& s) {
  float a = fmaxf(fmaxf(s[0], s[1]), s[2]), b = fmaxf(fmaxf(s[3], s[4]), s[5]);
  a = fmaxf(fmaxf(a, s[6]), s[7]); b = fmaxf(fmaxf(b, s[8]), s[9]); a = fmaxf(fmaxf(a, s[10]), s[11]); b = fmaxf(fmaxf(b, s[12]), s[13]);
  a = fmaxf(fmaxf(a, s[14]), s[15]); return fmaxf(a, b); }
#define EX2(x) __builtin_amdgcn_exp2f(x)

template <bool CONJ>
DI void cmul2(float a0, float b0, float c0, float s0, float a1, float b1, float c1, float s1, unsigned& re, unsigned& im) {
  float t0, t1, t2, t3;
  if (CONJ)
    asm("v_mul_f32 %2, %7, %9\n\tv_mul_f32 %3, %6, %9\n\tv_mul_f32 %4, %11, %13\n\tv_mul_f32 %5, %10, %13\n\t"
        "v_fma_f32 %2, %6, %8, %2\n\tv_fma_f32 %3, %7, %8, -%3\n\tv_fma_f32 %4, %10, %12, %4\n\tv_fma_f32 %5, %11, %12, -%5\n\t"
        "v_cvt_pk_bf16_f32 %0, %2, %4\n\tv_cvt_pk_bf16_f32 %1, %3, %5"
        : "=v"(re), "=v"(im), "=&v"(t0), "=&v"(t1), "=&v"(t2), "=&v"(t3) : "v"(a0), "v"(b0), "v"(c0), "v"(s0), "v"(a1), "v"(b1), "v"(c1), "v"(s1));
  else
    asm("v_mul_f32 %2, %7, %9\n\tv_mul_f32 %3, %6, %9\n\tv_mul_f32 %4, %11, %13\n\tv_mul_f32 %5, %10, %13\n\t"
        "v_fma_f32 %2, %6, %8, -%2\n\tv_fma_f32 %3, %7, %8, %3\n\tv_fma_f32 %4, %10, %12, -%4\n\tv_fma_f32 %5, %11, %12, %5\n\t"
        "v_cvt_pk_bf16_f32 %0, %2, %4\n\tv_cvt_pk_bf16_f32 %1, %3, %5"
        : "=v"(re), "=v"(im), "=&v"(t0), "=&v"(t1), "=&v"(t2), "=&v"(t3) : "v"(a0), "v"(b0), "v"(c0), "v"(s0), "v"(a1), "v"(b1), "v"(c1), "v"(s1));
}
#define MF_DONE(x, y) do { asm volatile("s_nop 11" : "+v"(x), "+v"(y)); SBAR(); } while (0)
#define VA_DONE(p, q, r_, s_) do { asm volatile("s_nop 1" : "+v"(p), "+v"(q), "+v"(r_), "+v"(s_)); SBAR(); } while (0)

template <class ASKW, class WAITW>
DI void a2_column(int c, int lane, const char* __restrict__ ws_in, char* __restrict__ ws, LDS_AS char* tl, ASKW&& ask_w, WAITW&& wait_w) {
  const int r = lane & 31, h = lane >> 5;
  int l16 = lane * 16;
  const bool real_col = (c == 0) || (c == 224);
  const int cp = c == 224 ? 1 : 2 * c;
  const rsrc_t rx = mkrsrc(ws_in + WS_X1 + (cp >> 5) * QH + (cp & 31) * 16, 0x40000u);
  const rsrc_t rt = mkrsrc(&g_A2, sizeof(g_A2)), rw = mkrsrc(&g_TW, sizeof(g_TW)), rf = mkrsrc(ws_in + WS_WT + c * 8192, 8192);
  const int xv = r * 2048 + h * 512;
#define FRESH() asm volatile("" : "+v"(l16))
#define LD_T32(base) do { _Pragma("unroll") for (int i = 0; i < 6; ++i) t32[i >> 1][i & 1] = asb(bld(rt, l16, (base) + i * 1024)); } while (0)
#define LD_T64(d, kh) do { _Pragma("unroll") for (int i = 0; i < 12; ++i) d[i >> 2][(i >> 1) & 1][i & 1] = asb(bld(rt, l16, 6144 + ((i >> 1) * 4 + 2 * (kh) + (i & 1)) * 1024)); } while (0)
#define LD_TW(m) do { _Pragma("unroll") for (int i = 0; i < 8; ++i) tw[m][i >> 1][i & 1] = __builtin_bit_cast(f32x4, bld(rw, l16, ((m) * 8 + i) * 1024)); } while (0)
  bf16x8 ur[2][2], ui[2][2], t32[3][2], ta[3][2][2], tb[3][2][2];
  f32x4 tw[2][4][2]; u32x4 wv[2][4], b[8];
  f32x16 re[2], im[2], xr[2], xi[2];
#pragma unroll
  for (int m = 0; m < 2; ++m)
#pragma unroll
    for (int t = 0; t < 2; ++t) { ur[m][t] = asb(bldh(rx, xv, m * 65536 + t * 1024)); ui[m][t] = asb(bldh(rx, xv, m * 65536 + t * 1024 + 16)); }
  LD_T32(0); LD_TW(0); LD_TW(1);
  SBAR();
  if (real_col) {
#pragma unroll
    for (int m = 0; m < 2; ++m)
#pragma unroll
      for (int t = 0; t < 2; ++t) ui[m][t] = (bf16x8){0, 0, 0, 0, 0, 0, 0, 0};
  }
  __builtin_amdgcn_s_setprio(2);
#define DFT32(i, R, I, ar, ai, S1, S2) do { constexpr int t_ = (i >> 1) & 1; \
    if (i < 4) { if (i & 1) I = MFMA(ai[t_], t32[0][t_], i < 2 ? zero16() : I); else R = MFMA(ar[t_], t32[0][t_], i < 2 ? zero16() : R); } \
    else { if (i & 1) I = MFMA(ar[t_], t32[S2][t_], I); else R = MFMA(ai[t_], t32[S1][t_], R); } } while (0)
#define DFT64(i, R, I, T, m, K0, S1, S2) do { constexpr int k_ = i >> 2; const bool z_ = (K0) == 0 && i < 2; \
    if ((i & 3) == 0) R = MFMA(T[0][m][k_], asb(b[K0 + k_]), z_ ? zero16() : R); else if ((i & 3) == 1) I = MFMA(T[0][m][k_], asb(b[4 + K0 + k_]), z_ ? zero16() : I); \
    else if ((i & 3) == 2) R = MFMA(T[S1][m][k_], asb(b[4 + K0 + k_]), R); else I = MFMA(T[S2][m][k_], asb(b[K0 + k_]), I); } while (0)
#define TWID(CONJ, i, R, I, m) do { constexpr int e = 2 * i; const f32x4 cs = tw[m][e >> 2][0], sn = tw[m][e >> 2][1]; unsigned pr, pi; \
    cmul2<CONJ>(R[e], I[e], cs[e & 3], sn[e & 3], R[e + 1], I[e + 1], cs[(e & 3) + 1], sn[(e & 3) + 1], pr, pi); \
    b[2 * (m) + (e >> 3)][(e >> 1) & 3] = pr; b[4 + 2 * (m) + (e >> 3)][(e >> 1) & 3] = pi; } while (0)
#define FILT(i, R, I, m) do { constexpr int e = 2 * i; const unsigned w0 = wv[m][e >> 2][e & 3], w1 = wv[m][e >> 2][(e & 3) + 1]; unsigned pr, pi; \
    cmul2<false>(R[e], I[e], __uint_as_float(w0 << 16), __uint_as_float(w0 & 0xffff0000u), R[e + 1], I[e + 1], __uint_as_float(w1 << 16), __uint_as_float(w1 & 0xffff0000u), pr, pi); \
    c2[2 * (m) + (e >> 3)][(e >> 1) & 3] = pr; c2[4 + 2 * (m) + (e >> 3)][(e >> 1) & 3] = pi; } while (0)
#define REP8(X) X(0) X(1) X(2) X(3) X(4) X(5) X(6) X(7)
#define X(i) DFT32(i, re[0], im[0], ur[0], ui[0], 1, 2);
  REP8(X)
#undef X
  SBAR(); LD_T64(ta, 0); ask_w(); MF_DONE(re[0], im[0]);
#define X(i) DFT32(i, re[1], im[1], ur[1], ui[1], 1, 2); SBAR(); TWID(true, i, re[0], im[0], 0); PIN(b[i >> 2]); PIN(b[4 + (i >> 2)]); SBAR();
  REP8(X)
#undef X
  VA_DONE(b[0], b[1], b[4], b[5]); MF_DONE(re[1], im[1]);
#define X(i) DFT64(i, xr[0], xi[0], ta, 0, 0, 1, 2); DFT64(i, xr[1], xi[1], ta, 1, 0, 1, 2); SBAR(); TWID(true, i, re[1], im[1], 1); PIN(b[2 + (i >> 2)]); PIN(b[6 + (i >> 2)]); SBAR();
  REP8(X)
#undef X
  LD_T64(tb, 1);
  wait_w();
#pragma unroll
  for (int i = 0; i < 8; ++i) wv[i >> 2][i & 3] = bldh(rf, l16, i * 1024);
  VA_DONE(b[2], b[3], b[6], b[7]);
#define X(i) DFT64(i, xr[0], xi[0], tb, 0, 2, 1, 2);
  REP8(X)
#undef X
  u32x4 c2[8];
  SBAR(); FRESH(); LD_T64(ta, 0);
  MF_DONE(xr[0], xi[0]);
#define X(i) DFT64(i, xr[1], xi[1], tb, 1, 2, 1, 2); SBAR(); FILT(i, xr[0], xi[0], 0); PIN(c2[i >> 2]); PIN(c2[4 + (i >> 2)]); SBAR();
  REP8(X)
#undef X
  b[0] = c2[0]; b[1] = c2[1]; b[4] = c2[4]; b[5] = c2[5];
  VA_DONE(b[0], b[1], b[4], b[5]); MF_DONE(xr[1], xi[1]);
#define X(i) DFT64(i, re[0], im[0], ta, 0, 0, 2, 1); DFT64(i, re[1], im[1], ta, 1, 0, 2, 1); SBAR(); FILT(i, xr[1], xi[1], 1); PIN(c2[2 + (i >> 2)]); PIN(c2[6 + (i >> 2)]); SBAR();
  REP8(X)
#undef X
  b[2] = c2[2]; b[3] = c2[3]; b[6] = c2[6]; b[7] = c2[7];
  LD_T64(tb, 1); LD_TW(0); LD_TW(1); LD_T32(6144 + 24576); VA_DONE(b[2], b[3], b[6], b[7]);
#define X(i) DFT64(i, re[0], im[0], tb, 0, 2, 2, 1);
  REP8(X)
#undef X
  MF_DONE(re[0], im[0]);
  LDS_AS char* tq = tl + r * 192 + 8 * h;
#define TW5(i, m) do { TWID(false, i, re[m], im[m], m); \
    if (i & 1) { *(LDS_AS u32x2*)(tq + 64 * (m) + 16 * (i >> 1)) = (u32x2){b[2 * (m) + (i >> 2)][(i - 1) & 3], b[2 * (m) + (i >> 2)][i & 3]}; \
                 *(LDS_AS u32x2*)(tq + 6144 + 64 * (m) + 16 * (i >> 1)) = (u32x2){b[4 + 2 * (m) + (i >> 2)][(i - 1) & 3], b[4 + 2 * (m) + (i >> 2)][i & 3]}; } } while (0)
  { const u32x4 k2 = b[2], k3 = b[3], k6 = b[6], k7 = b[7];
#define X(i) DFT64(i, re[1], im[1], tb, 1, 2, 2, 1); SBAR(); TW5(i, 0); SBAR();
    REP8(X)
#undef X
    (void)k2; (void)k3; (void)k6; (void)k7; }
  asm volatile("s_waitcnt lgkmcnt(0)" ::: "memory");
  const LDS_AS char* tp = tl + (8 * h + ((lane & 15) >> 2)) * 192 + ((lane >> 4) & 1) * 32 + (lane & 3) * 8;
#define LD_TR(m) do { _Pragma("unroll") for (int t = 0; t < 2; ++t) { ur[m][t] = ldtr2(tp + t * 16 * 192 + (m) * 64, 4 * 192); ui[m][t] = ldtr2(tp + 6144 + t * 16 * 192 + (m) * 64, 4 * 192); } } while (0)
  LD_TR(0);
  MF_DONE(re[1], im[1]);
#define X(i) DFT32(i, xr[0], xi[0], ur[0], ui[0], 2, 1); SBAR(); TW5(i, 1); SBAR();
  REP8(X)
#undef X
  asm volatile("s_waitcnt lgkmcnt(0)" ::: "memory");
  LD_TR(1);
  const rsrc_t rz = mkrsrc(ws + WS_Z2 + c * 8192, 8192);
#define ZST(m, g) bsth((u32x4){pk(xr[m][4 * (g)], xi[m][4 * (g)]), pk(xr[m][4 * (g) + 1], xi[m][4 * (g) + 1]), pk(xr[m][4 * (g) + 2], xi[m][4 * (g) + 2]), pk(xr[m][4 * (g) + 3], xi[m][4 * (g) + 3])}, rz, lane * 16, ((m) * 4 + (g)) * 1024)
  SBAR();
#define X(i) DFT32(i, xr[1], xi[1], ur[1], ui[1], 2, 1); SBAR(); if (i >= 4) { ZST(0, (i - 4) & 3); SBAR(); }
  REP8(X)
#undef X
#pragma unroll
  for (int g = 0; g < 4; ++g) ZST(1, g);
}

constexpr int K2_EX = 0, EXT = 3840, K2_SW = 12 * EXT, K3_WO = 49152, K3_F = K3_WO + 57344, K2_TL = K3_F + 57344 - 12288;

#define A_MF(d, a, b, c) "v_mfma_f32_32x32x16_bf16 v[" #d ":" #d "+15], v[" #a ":" #a "+3], v[" #b ":" #b "+3], " c "\n\t"
#define A_ACC(d) "v[" #d ":" #d "+15]"
#define A_EXP(s, p, X) X "v_exp_f32 v184, v[" #s "]\n\tv_exp_f32 v185, v[" #s "+1]\n\tv_exp_f32 v186, v[" #s "+2]\n\tv_exp_f32 v187, v[" #s "+3]\n\t" \
                    "v_cvt_pk_bf16_f32 v[" #p "], v184, v185\n\tv_cvt_pk_bf16_f32 v[" #p "+1], v186, v187\n\t"
#define A_PERIOD(SC, SN, KN, QN, OC, V0C, OP, V1P, XA, XB, XC, XD) \
  A_MF(SN, KN, QN, "0")                 A_EXP(SC, 176, XA) \
  A_MF(OP, V1P, 180, A_ACC(OP))         A_EXP(SC + 4, 178, XB) \
  A_MF(SN, KN + 4, QN + 4, A_ACC(SN))   A_EXP(SC + 8, 180, XC) \
  A_MF(OC, V0C, 176, A_ACC(OC))         A_EXP(SC + 12, 182, XD)
#define A_LD(d, so, off) "global_load_dwordx4 v[" #d ":" #d "+3], v188, " so " offset:" #off " sc1\n\t"
#define A_TILE(C, N, F, PC, PC0, PC1, PF) \
  A_PERIOD(64, 80, C, 104, 0, C + 8, 48, F + 12, "", A_LD(F + 8, PF, 2048) A_LD(F + 12, PF, 3072), "s_waitcnt vmcnt(8)\n\t", "") \
  A_PERIOD(80, 64, C, 112, 16, C + 8, 0, C + 12, "s_add_u32 %[off], %[off], 0x1000\n\ts_cmp_eq_u32 %[cnt], -3\n\ts_cselect_b32 %[off], 0x3e000, %[off]\n\t", "s_add_u32 " PC0 ", %[kvlo], %[off]\n\ts_addc_u32 " PC1 ", %[kvhi], 0\n\t", "", "") \
  A_PERIOD(64, 80, C, 120, 32, C + 8, 16, C + 12, "", "", "", "s_waitcnt vmcnt(6)\n\t") \
  A_PERIOD(80, 64, N, 96, 48, C + 8, 32, C + 12, A_LD(C, PC, 0) A_LD(C + 4, PC, 1024), "", "", "s_add_u32 %[cnt], %[cnt], 1\n\t")

#define A_W16(a, o, d) "ds_write_b128 v" #a ", v[" #d ":" #d "+3] offset:" #o "\n\t"
#define A_W8(a, d) "ds_write_b64 v" #a ", v[" #d ":" #d "+1]\n\ts_mov_b32 exec_lo, 0\n\tds_write_b64 v" #a ", v[" #d "+2:" #d "+3] offset:256\n\ts_mov_b32 exec_lo, -1\n\t"
#define A_TAIL(C, N, F) \
  "s_waitcnt vmcnt(4)\n\t" \
  A_PERIOD(64, 80, F, 96, 0, N + 8, 48, C + 12, A_W16(189, 0, 16) A_W16(189, 1024, 20), A_W16(189, 2048, 24) A_W8(190, 28), A_W16(191, 0, 32) A_W16(191, 1024, 36), A_W16(191, 2048, 40) A_W8(192, 44)) \
                                    A_EXP(80, 176, "") \
  A_MF(0, N + 12, 180, A_ACC(0))    A_EXP(84, 178, A_W16(193, 0, 48) A_W16(193, 1024, 52) A_W16(193, 2048, 56)) \
                                    A_EXP(88, 180, "s_waitcnt vmcnt(2)\n\t") \
  A_MF(0, F + 8, 176, A_ACC(0))     A_EXP(92, 182, A_W8(194, 60)) \
  "s_nop 1\n\t" A_MF(0, F + 12, 180, A_ACC(0))

DI void p3_tokens_a(u32x4 (&tk)[16], const char* ws, int tt, int kh, int lane) {
  const int r = lane & 31, h = lane >> 5;
  const rsrc_t rs = mkrsrc(ws + WS_Z2 + tt * 128 + kh * 112 * 8192, 2u << 20);
#pragma unroll
  for (int s = 0; s < 14; ++s)
#pragma unroll
    for (int i = 0; i < 4; ++i) tk[s][i] = __builtin_amdgcn_raw_buffer_load_b32(rs, h * 4 * 8192 + r * 4, (8 * s + i) * 8192, LD_AUX);
  tk[14][0] = __builtin_amdgcn_raw_buffer_load_b32(rs, r * 4, kh ? 0 : 224 * 8192, LD_AUX);
}
DI void p3_mma_a(u32x4 (&tk)[16], const LDS_AS char* lds, int kh, int lane, f32x16& acc0, f32x16& acc1) {
  const int r = lane & 31, h = lane >> 5, sw = (r >> 1) & 7;
  tk[0][0] = (h == 0 && kh == 0) ? (tk[0][0] & 0xffffu) | (tk[14][0] << 16) : tk[0][0];
  const LDS_AS char* wa = lds + K3_F + r * 128 + kh * 3 * 8192;
  int xo[4];
#pragma unroll
  for (int i = 0; i < 4; ++i) xo[i] = ((2 * ((i + 2 * kh) & 3) + h) ^ sw) * 16 + ((i + 2 * kh) >> 2) * 8192;
  bf16x8 fa[3], fb[3];
#define P3A(s) (wa + ((s) >> 2) * 8192 + xo[(s) & 3])
  fa[0] = ldsr(P3A(0)); fb[0] = ldsr(P3A(0) + 4096); fa[1] = ldsr(P3A(1)); fb[1] = ldsr(P3A(1) + 4096);
  acc0 = zero16(); acc1 = zero16();
  SBAR();
#pragma unroll
  for (int s = 0; s < 14; ++s) {
    acc0 = MFMA(fa[s % 3], asb(tk[s]), acc0); SBAR();
    if (s + 2 < 14) { fa[(s + 2) % 3] = ldsr(P3A(s + 2)); SBAR(); }
    acc1 = MFMA(fb[s % 3], asb(tk[s]), acc1); SBAR();
    if (s + 2 < 14) { fb[(s + 2) % 3] = ldsr(P3A(s + 2) + 4096); SBAR(); }
  }
}
DI void p3_tokens_b(u32x4 (&tk)[16], const char* p, int lane) {
  const rsrc_t rs = mkrsrc(p, 16384);
#pragma unroll
  for (int s = 0; s < 16; ++s) tk[s] = bldh(rs, lane * 16, s * 1024);
}
DI void p3_mma_b(u32x4 (&tk)[16], const LDS_AS char* lds, int kh, int lane, f32x16& acc0, f32x16& acc1) {
  const int h = lane >> 5, q = (lane & 15) >> 2;
  const LDS_AS char* w0 = lds + K3_WO + kh * 8 * 3584 + (4 * h + q) * 128 + ((q >> 1) * 64) + ((lane >> 4) & 1) * 32 + (lane & 3) * 8;
  const LDS_AS char* w1 = lds + K3_WO + kh * 8 * 3584 + (4 * h + q) * 128 + (((q >> 1) ^ 1) * 64) + ((lane >> 4) & 1) * 32 + (lane & 3) * 8;
  bf16x8 wa[3], wb[3];
#define P3B(s) (((s) >> 1) * 3584 + ((s) & 1) * 2048), (((s) & 1) ? 512 : 1024)
  wa[0] = ldtr2(w0 + P3B(0)); wb[0] = ldtr2(w1 + P3B(0)); wa[1] = ldtr2(w0 + P3B(1)); wb[1] = ldtr2(w1 + P3B(1));
  acc0 = zero16(); acc1 = zero16();
  SBAR();
#pragma unroll
  for (int s = 0; s < 16; ++s) {
    acc0 = MFMA(wa[s % 3], asb(tk[s]), acc0); SBAR();
    if (s + 2 < 16) { wa[(s + 2) % 3] = ldtr2(w0 + P3B(s + 2)); SBAR(); }
    acc1 = MFMA(wb[s % 3], asb(tk[s]), acc1); SBAR();
    if (s + 2 < 16) { wb[(s + 2) % 3] = ldtr2(w1 + P3B(s + 2)); SBAR(); }
  }
}
DI f32x16 p3_give(LDS_AS char* red, int wave, int kh, int lane, const f32x16& acc0, const f32x16& acc1) {
  LDS_AS char* p = red + wave * 4096 + lane * 16;
#pragma unroll
  for (int g = 0; g < 4; ++g)
    *(LDS_AS f32x4*)(p + g * 1024) = kh ? (f32x4){acc0[4 * g], acc0[4 * g + 1], acc0[4 * g + 2], acc0[4 * g + 3]} : (f32x4){acc1[4 * g], acc1[4 * g + 1], acc1[4 * g + 2], acc1[4 * g + 3]};
  f32x16 k;
#pragma unroll
  for (int i = 0; i < 16; ++i) k[i] = kh ? acc1[i] : acc0[i];
  return k;
}
DI void p3_stage(const LDS_AS char* red, LDS_AS char* stg, int w, int lane, const f32x16& keep) {
  const LDS_AS char* pp = red + (w ^ 2) * 4096 + lane * 16;
  LDS_AS char* q = stg + (w & 1) * 4608 + (lane & 31) * 144 + (w >> 1) * 64 + (lane >> 5) * 8;
#pragma unroll
  for (int g = 0; g < 4; ++g) { const f32x4 v = *(const LDS_AS f32x4*)(pp + g * 1024);
    *(LDS_AS u32x2*)(q + g * 16) = (u32x2){pk(keep[4 * g] + v[0], keep[4 * g + 1] + v[1]), pk(keep[4 * g + 2] + v[2], keep[4 * g + 3] + v[3])}; }
}
template <bool SPEC>
DI void p3_rows(const LDS_AS char* stg, int w, int lane, int tt3, u16* out, int colb) {
#pragma unroll
  for (int i = 0; i < 2; ++i) {
    const int row = 16 * (w >> 1) + 8 * i + (lane >> 3), q = 32 * tt3 + row, m2 = (q >> 2) & 63, g2 = q >> 8;
    const int tok = SPEC ? 64 * (m2 & 31) + 32 * (g2 >> 2) + 8 * (g2 & 3) + 4 * (m2 >> 5) + (q & 3) : q;
    const u32x4 v = *(const LDS_AS u32x4*)(stg + (w & 1) * 4608 + row * 144 + (lane & 7) * 16);
    __builtin_amdgcn_raw_buffer_store_b128(v, mkrsrc(out, 2048 * 1792), tok * 1792 + colb + (lane & 7) * 16, 0, 16);
  }
}

enum { SW_PZ = 0, SW_IN = 1, SW_EX = 2, SW_OD = 3, SW_PO = 4, SW_W3 = 5, SW_RA = 6, SW_RB = 7, SW_FL = 8, SW_FS = 9, SW_TAG = 10 };
DI void sw_add(LDS_AS char* lds, int w) { asm volatile("s_mov_b64 exec, 1\n\tds_add_u32 %0, %1\n\ts_mov_b64 exec, -1" :: "v"((unsigned)(uintptr_t)(lds + K2_SW + 4 * w)), "v"(1) : "memory"); }
DI int sw_get(LDS_AS char* lds, int w) { int v; asm volatile("ds_read_b32 %0, %1\n\ts_waitcnt lgkmcnt(0)" : "=v"(v) : "v"((unsigned)(uintptr_t)(lds + K2_SW + 4 * w)) : "memory"); return __builtin_amdgcn_readfirstlane(v); }
DI int sw_inc(LDS_AS char* lds, int w) { int v; asm volatile("s_mov_b64 exec, 1\n\tds_add_rtn_u32 %0, %1, %2\n\ts_waitcnt lgkmcnt(0)\n\ts_mov_b64 exec, -1" : "=&v"(v) : "v"((unsigned)(uintptr_t)(lds + K2_SW + 4 * w)), "v"(1) : "memory"); return __builtin_amdgcn_readfirstlane(v); }
template <int SLEEP> DI void sw_wait(LDS_AS char* lds, int w, int n) {
#pragma unroll 1
  for (int spin = 0; spin < (1 << 22) && sw_get(lds, w) < n; ++spin) if (SLEEP) __builtin_amdgcn_s_sleep(SLEEP); }

__global__ __launch_bounds__(512, 2) void fused(const u16* __restrict__ ax, const u16* __restrict__ wr, const u16* __restrict__ wi,
                                                const u16* __restrict__ bx, const u16* __restrict__ wqkv, const u16* __restrict__ wo,
                                                u16* __restrict__ out, char* __restrict__ ws) {
  __shared__ __attribute__((aligned(1024))) char smem[163840];
  LDS_AS char* lds = (LDS_AS char*)smem;
  const int bid = blockIdx.x, xcd = bid & 7, jj = bid >> 3;
  const int lane = threadIdx.x & 63, wave = __builtin_amdgcn_readfirstlane(threadIdx.x >> 6);
  const int mt = 8 * (xcd & 1) + (jj & 7), nt = 4 * (xcd >> 1) + (jj >> 3), type = nt >> 2;
  const int head = 2 * xcd + (jj & 1), qb = jj >> 1, col = (jj >> 2) * 32 + xcd * 4 + (jj & 3);
  const int mt3 = 4 * xcd + (jj & 3), nt3 = jj >> 2;

  if (__builtin_expect(wave >= 4, 1)) {
    const u64 nonce = __hip_atomic_load((gu64*)(ws + WS_SY), RLX_AGENT);
    LDS_AS char* fs = lds + 131072 + 1024;
    k1_loader(lds, lane, wave - 4, mt, nt, ax, bx, wqkv, [&]() { filt_dma(wave - 4, lane, bid, wr, wi, (unsigned)(uintptr_t)fs); });
    const u64 tag = nonce + 1;
    if (wave == 5 && lane < 12) *(LDS_AS unsigned*)(lds + K2_SW + 4 * lane) = lane < SW_TAG ? 0u : lane == SW_TAG ? (unsigned)tag : (unsigned)(tag >> 32);
    BARRIER();
    if (wave == 5) { flag_set(ws, SY_P1 + nt * 16 + mt, tag, lane); WAITV(1); } else WAITV(0);

    sw_add(lds, SW_FL);
    sw_wait<1>(lds, SW_FL, 4);
    u32x4 fq[2]; int fo[2];
    filt_gather(threadIdx.x - 256, bid, fs, fq, fo);
    PIN(fq[0]); PIN(fq[1]);
    const rsrc_t rt = mkrsrc(ws + WS_WT, 2u << 20);
    if (wave == 4) { bsth(fq[0], rt, fo[0], 0); bsth(fq[1], rt, fo[1], 0); }
    if (wave == 5) {
      const int g = head >> 2, gx = col < 224 ? 12 + (col >> 6) : 12;
      const int off[1] = {SY_P1 + (lane < 48 ? (lane < 16 ? 4 + g : lane < 32 ? 8 + g : gx) * 16 + (lane & 15) : g * 16 + qb)};
      flag_wait(ws, off, tag); sw_add(lds, SW_IN);
    } else sw_wait<4>(lds, SW_IN, 1);
    if (wave != 4) { bsth(fq[0], rt, fo[0], 0); bsth(fq[1], rt, fo[1], 0); }
    WAITV(0);
    if (sw_inc(lds, SW_FS) == 3) flag_set(ws, SY_PW + (bid & 3) * 64 + (bid >> 2), tag, lane);
    if (wave == 5) {
      if (bid == 0) {
        const int o4[4] = {SY_P1 + lane, SY_P1 + 64 + lane, SY_P1 + 128 + lane, SY_P1 + 192 + lane};
        flag_wait(ws, o4, tag);
        flag_set(ws, 0, tag, lane);
      }
      if (nt3 == 7) return;
      __builtin_amdgcn_s_sleep(127);
    } else {
    const int rr = lane >> 3, sl = lane & 7;
    const char* gf = (const char*)&g_F + (64 * nt3 + rr) * 896;
    const unsigned vf = (sl ^ (rr >> 1)) * 16;
    if (wave == 4) {
      if (col < DF) {
        const int ow[1] = {SY_PW + (col * 1150 >> 16) * 64 + lane};
        u64 seen = 0;
        a2_column(col, lane, ws, ws, lds + K2_TL, [&]() { seen = __hip_atomic_load((gu64*)(ws + WS_SY + sy_off(ow[0])), RLX_AGENT); },
                  [&]() { if (!__all(seen == tag)) flag_wait(ws, ow, tag); __builtin_amdgcn_fence(__ATOMIC_ACQUIRE, "wavefront"); });
        WAITV(0);
        flag_set(ws, SY_PZ + col, tag, lane);
      }
      if (nt3 == 7) return;
#pragma unroll 1
      for (int p = 44; p < 56; ++p) dma16(gf + (p & 7) * 8 * 896 + (p >> 3) * 128 + (vf ^ ((p & 1) * 64)), lds + K3_F + p * 1024);
    } else {
      if (nt3 == 7) return;
      __builtin_amdgcn_s_setprio(1);
      __builtin_amdgcn_s_sleep(40);
      if (wave == 6) {
        const char* g = (const char*)wo + nt3 * 128 + rr * 896 + (((sl >> 2) ^ ((rr >> 1) & 1)) * 64) + (sl & 3) * 16;
#pragma unroll 1
        for (int p = 0; p < 56; ++p) { dma16(g + p * 8 * 896, lds + K3_WO + p * 1024); __builtin_amdgcn_s_sleep(2); }
      } else {
        sw_wait<4>(lds, SW_FS, 4);
#pragma unroll 1
        for (int p = 0; p < 44; p += 2) {
          const char* g = gf + (p & 7) * 8 * 896 + (p >> 3) * 128;
          dma16(g + vf, lds + K3_F + p * 1024); __builtin_amdgcn_s_sleep(2);
          dma16(g + 8 * 896 + (vf ^ 64), lds + K3_F + p * 1024 + 1024); __builtin_amdgcn_s_sleep(2);
        }
      }
    }
    WAITV(0);
    sw_add(lds, SW_W3);
    if (wave == 4) {
      const int oz[4] = {SY_PZ + lane, SY_PZ + 64 + lane, SY_PZ + 128 + lane, SY_PZ + (lane < 33 ? 192 + lane : 192)};
      flag_wait(ws, oz, tag);
      sw_add(lds, SW_PZ);
    }
    }
    {
      __builtin_amdgcn_s_setprio(0);
      const int w = wave - 4, tt3 = 2 * mt3 + (w & 1), kh = w >> 1;
      u32x4 tk[16]; f32x16 acc0, acc1;
      sw_wait<8>(lds, SW_PZ, 1);
      p3_tokens_a(tk, ws, tt3, kh, lane);
      sw_wait<1>(lds, SW_W3, 3);
      sw_wait<2>(lds, SW_EX, 1);
      p3_mma_a(tk, lds, kh, lane, acc0, acc1);
      sw_add(lds, SW_RA); sw_wait<0>(lds, SW_RA, 4);
      const f32x16 keep = p3_give(lds + K3_F, w, kh, lane, acc0, acc1);
      sw_add(lds, SW_RA); sw_wait<0>(lds, SW_RA, 8);
      p3_stage(lds + K3_F, lds + K3_F + 16384, w, lane, keep);
      sw_add(lds, SW_RA); sw_wait<0>(lds, SW_RA, 12);
      p3_rows<true>(lds + K3_F + 16384, w, lane, tt3, out, nt3 * 128);
    }
    return;
  }
  const int r = lane & 31, h = lane >> 5;

  {
    const int wm = wave & 1, wn = wave >> 1;
  const unsigned scr = (unsigned)(uintptr_t)(lds + 131072);
  {
    int i = wave * 64 + lane; i = i > 223 ? 223 : i;
    const int L = (nt & 3) * 224 + i, row = (L * 9363) >> 16, seg = L - 7 * row;
    const int tok = type == 3 ? 64 * (row & 31) + 4 * mt + (row >> 5) : 128 * mt + row;
    touch((const char*)(type == 3 ? ax : bx) + tok * 896 + seg * 128, scr);
    int j = wave * 64 + lane; j = j > 167 ? 167 : j;
    const int wrow = (j * 21846) >> 16, ls = j - 3 * wrow;
    const int woff = (56 * (mt & 7) + wrow) * (type == 3 ? 896 : 2688) + (ls == 2 ? 220 : ls * 128);
    const char* tp = (type == 3 ? (const char*)&g_F + (nt - 12) * 256 : (const char*)wqkv + nt * 224) + woff;
    touch(tp, scr);
  }
  const int tsz = (type == 0 || type == 3) ? 2048 : 4096, hsz = (type == 0 || type == 3) ? QH : KH;
  const char* dbase = ws + (type == 0 ? WS_QF : type == 3 ? WS_X1 : WS_KV + (type == 2 ? 2048 : 0)) + (4 * (nt & 3) + 2 * wn) * hsz + (4 * mt + 2 * wm) * tsz;
  int dv[4] = {lane * 16, lane * 16 + hsz, lane * 16 + tsz, lane * 16 + hsz + tsz};
  asm volatile("" : "+s"(dbase)); asm volatile("" : "+v"(dv[0]), "+v"(dv[1]), "+v"(dv[2]), "+v"(dv[3]));
  const rsrc_t rd = mkrsrc(dbase, 1u << 20);
  f32x16 acc[2][2];
  auto epi = [&](auto KIND) {

    float qs = type == 0 ? 0.18898223650461362f * 1.4426950408889634f : 1.0f;
    const unsigned kone = type == 1 ? 0x3f80u : 0u, vone = r == 24 ? 0x3f803f80u : 0u;
    const bool padv = (r >> 2) == 6;
#pragma unroll
    for (int i = 0; i < 4; ++i) {
      f32x16 v = acc[i >> 1][i & 1];
      if (KIND.value == 0) {
#pragma unroll
        for (int e = 0; e < 16; ++e) v[e] *= qs;
      }
      u32x4 p0 = pk8(v, 0), p1 = pk8(v, 1);
      if (KIND.value == 0 && h == 0) { p1[2] = kone; p1[3] = 0; }
      if (KIND.value == 1 && padv) { p0 = (u32x4){vone, vone, vone, vone}; p1 = p0; }
      bsth(p0, rd, dv[i], 0); bsth(p1, rd, dv[i], 1024);
    } };
  if (type < 2) { k1_loop<true, 224>(lds, lane, wm, wn, acc); epi(IntC<0>{}); }
  else if (type == 2) { k1_loop<false, 224>(lds, lane, wm, wn, acc); epi(IntC<1>{}); }
  else { k1_loop<false, 256>(lds, lane, wm, wn, acc); if (!(nt == 15 && wn == 1)) epi(IntC<2>{}); }
  }
  DRAINBAR();

  const int tt3 = 2 * mt3 + (wave & 1), kh = wave >> 1;
  u32x4 tk[16];
  const char* tokp = ws + WS_OF + tt3 * OQ + kh * 16384;
  const char* pop = ws + WS_SY + sy_off(SY_PO + qb * 16 + head);
  const char* pwp = ws + WS_SY + sy_off(SY_PO + (mt3 >> 1) * 16);
  {
  const int n0 = col < DF ? 11 : 16, no = (62 - n0 + 2) / 3;
  const int st = wave == 0 ? 0 : n0 + (wave - 1) * no, ntl0 = wave == 0 ? n0 : (wave == 3 ? 62 - st : no);
  const char* kvp = ws + WS_KV + head * KH; const char* qp = ws + WS_QF + head * QH + 4 * qb * 2048;
  int offA = st << 12, offB = offA + 4096;
  int offC = offA + 8192;
  const int l16 = lane * 16;
  int ntl = ntl0, qo[4] = {wave * 2048, ((wave + 1) & 3) * 2048, ((wave + 2) & 3) * 2048, ((wave + 3) & 3) * 2048};
  f32x16 O[4] = {zero16(), zero16(), zero16(), zero16()};
  unsigned xa[3], xb[3];
#pragma unroll
  for (int t = 1; t < 4; ++t) { const unsigned p = (unsigned)(uintptr_t)(lds + K2_EX) + (((wave + t) & 3) * 3 + t - 1) * EXT; xa[t - 1] = p + l16; xb[t - 1] = p + 3072 + lane * 8; }
  asm volatile("" : "+{v189}"(xa[0]), "+{v190}"(xb[0]), "+{v191}"(xa[1]), "+{v192}"(xb[1]), "+{v193}"(xa[2]), "+{v194}"(xb[2]));
  asm volatile("" : "+s"(kvp), "+s"(qp), "+s"(offA), "+s"(offB), "+s"(offC), "+s"(ntl), "+s"(qo[0]), "+s"(qo[1]), "+s"(qo[2]), "+s"(qo[3]));
  asm volatile("" : "+{v[0:15]}"(O[0]), "+{v[16:31]}"(O[1]), "+{v[32:47]}"(O[2]), "+{v[48:63]}"(O[3]));
  const char* op = ws + WS_OF + (4 * qb + wave) * OQ + head * 2048;
  asm volatile("" : "+s"(op), "+s"(tokp), "+s"(pop), "+s"(pwp));
  const rsrc_t rkv = mkrsrc(kvp, 262144), rq = mkrsrc(qp, 8192), ro = mkrsrc(op, 2048);
  sw_wait<1>(lds, SW_IN, 1);
  u32x4 KA0 = bldh(rkv, l16, offA), KA1 = bldh(rkv, l16 + 1024, offA);
  u32x4 Q0[4], Q1[4];
#pragma unroll
  for (int t = 0; t < 4; ++t) { Q0[t] = bldh(rq, l16, qo[t]); Q1[t] = bldh(rq, l16 + 1024, qo[t]); }
  u32x4 VA0 = bldh(rkv, l16 + 2048, offA), VA1 = bldh(rkv, l16 + 3072, offA), KB0 = bldh(rkv, l16, offB), KB1 = bldh(rkv, l16 + 1024, offB);
  u32x4 VB0, VB1, KC0, KC1;
  float mref[4];
#pragma unroll
  for (int t = 0; t < 4; ++t) {
    f32x16 s = MFMA(asb(KA0), asb(Q0[t]), zero16()); s = MFMA(asb(KA1), asb(Q1[t]), s);
    const float m = swapmax(max16(s));
    const unsigned nb = pk(-m, 0.f);
    mref[t] = -__uint_as_float(nb << 16);
    Q1[t][2] = h == 0 ? nb : Q1[t][2];
    if (t) O[t][13] = h ? 0.f : mref[t];
  }

  u32x4 VC0, VC1 = {0, 0, 0, 0};
  {
    f32x16 SA = MFMA(asb(KA0), asb(Q0[0]), zero16()); SA = MFMA(asb(KA1), asb(Q1[0]), SA);
    int cnt = -ntl;
    const uint64_t kvs = (uint64_t)kvp;
    u32x4 P1 = {0, 0, 0, 0};
    asm volatile(
      "s_add_u32 s42, %[kvlo], %[offb]\n\ts_addc_u32 s43, %[kvhi], 0\n\ts_add_u32 s44, %[kvlo], %[off]\n\ts_addc_u32 s45, %[kvhi], 0\n\t"
      A_LD(152, "s[42:43]", 2048) A_LD(156, "s[42:43]", 3072) A_LD(160, "s[44:45]", 0) A_LD(164, "s[44:45]", 1024) "s_nop 3\n"
      ".p2align 6\n"
      "Ltop%=:\n\t"
      A_TILE(128, 144, 160, "s[40:41]", "s40", "s41", "s[44:45]")
      "s_cbranch_scc1 LendA%=\n\t"
      A_TILE(144, 160, 128, "s[42:43]", "s42", "s43", "s[40:41]")
      "s_cbranch_scc1 LendB%=\n\t"
      A_TILE(160, 128, 144, "s[44:45]", "s44", "s45", "s[42:43]")
      "s_cbranch_scc0 Ltop%=\n\t"
      A_TAIL(160, 128, 144)
      "s_branch Ldone%=\n"
      "LendA%=:\n\t"
      A_TAIL(128, 144, 160)
      "s_branch Ldone%=\n"
      "LendB%=:\n\t"
      A_TAIL(144, 160, 128)
      "Ldone%=:\n\t"
      "s_nop 11"
      : "+{v[0:15]}"(O[0]), "+{v[16:31]}"(O[1]), "+{v[32:47]}"(O[2]), "+{v[48:63]}"(O[3]), "+{v[64:79]}"(SA),
        "+{v[128:131]}"(KA0), "+{v[132:135]}"(KA1), "+{v[136:139]}"(VA0), "+{v[140:143]}"(VA1), "+{v[144:147]}"(KB0), "+{v[148:151]}"(KB1), "={v[152:155]}"(VB0), "={v[156:159]}"(VB1),
        "={v[160:163]}"(KC0), "={v[164:167]}"(KC1), "={v[168:171]}"(VC0), "+{v[172:175]}"(VC1), "+{v[180:183]}"(P1), [off] "+s"(offC), [cnt] "+s"(cnt)
      : "{v[96:99]}"(Q0[0]), "{v[100:103]}"(Q1[0]), "{v[104:107]}"(Q0[1]), "{v[108:111]}"(Q1[1]), "{v[112:115]}"(Q0[2]), "{v[116:119]}"(Q1[2]), "{v[120:123]}"(Q0[3]), "{v[124:127]}"(Q1[3]),
        "{v188}"(l16), "{v189}"(xa[0]), "{v190}"(xb[0]), "{v191}"(xa[1]), "{v192}"(xb[1]), "{v193}"(xa[2]), "{v194}"(xb[2]), [offb] "s"(offB), [kvlo] "s"((unsigned)kvs), [kvhi] "s"((unsigned)(kvs >> 32))
      : "memory", "scc", "s40", "s41", "s42", "s43", "s44", "s45", "v80", "v81", "v82", "v83", "v84", "v85", "v86", "v87", "v88", "v89", "v90", "v91", "v92", "v93", "v94", "v95",
        "v176", "v177", "v178", "v179", "v184", "v185", "v186", "v187");
  }

  sw_add(lds, SW_EX);

  sw_wait<0>(lds, SW_EX, 4);

  {
    const LDS_AS char* e = lds + K2_EX + wave * 3 * EXT;
    f32x2 lm[3], u[3], w[3]; f32x4 v[3][3];
#pragma unroll
    for (int k = 0; k < 3; ++k) lm[k] = *(const LDS_AS f32x2*)(e + k * EXT + 3072 + r * 8);
#pragma unroll
    for (int k = 0; k < 3; ++k) {
#pragma unroll
      for (int g = 0; g < 3; ++g) v[k][g] = *(const LDS_AS f32x4*)(e + k * EXT + g * 1024 + l16);
      u[k] = *(const LDS_AS f32x2*)(e + k * EXT + 3072 + lane * 8); w[k] = *(const LDS_AS f32x2*)(e + k * EXT + 3584 + r * 8);
    }
    SBAR();
    f32x16 acc = O[0];
    const float l0 = h ? 1.f : acc[12];
    const float M = fmaxf(fmaxf(mref[0], lm[0][1]), fmaxf(lm[1][1], lm[2][1]));
    const float fo = EX2(mref[0] - M);
#pragma unroll
    for (int i = 0; i < 16; ++i) acc[i] *= fo;
#pragma unroll
    for (int k = 0; k < 3; ++k) { const float f = EX2(lm[k][1] - M);
#pragma unroll
      for (int i = 0; i < 12; ++i) acc[i] = fmaf(v[k][i >> 2][i & 3], f, acc[i]);
      acc[12] = fmaf(u[k][0], f, acc[12]); acc[13] = fmaf(u[k][1], f, acc[13]); acc[14] = fmaf(w[k][0], f, acc[14]); acc[15] = fmaf(w[k][1], f, acc[15]); }
    auto put = [&](const f32x16& x) {
      auto rr = __builtin_amdgcn_permlane32_swap(__float_as_uint(x[12]), __float_as_uint(x[12]), false, false);
      const float inv = __builtin_amdgcn_rcpf(__uint_as_float(rr[0]));
      f32x16 y;
#pragma unroll
      for (int i = 0; i < 16; ++i) y[i] = x[i] * inv;
      u32x4 p0 = pk8(y, 0), p1 = pk8(y, 1);
      if (h == 0) { p1[2] = 0; p1[3] = 0; }
      bsth(p0, ro, l16, 0); bsth(p1, ro, l16, 1024); };
    put(acc);
    SBAR();
    const f32x4 s4 = (f32x4){acc[0], acc[1], acc[2], acc[3]} + (f32x4){acc[4], acc[5], acc[6], acc[7]} + ((f32x4){acc[8], acc[9], acc[10], acc[11]} + (f32x4){acc[12], acc[13], acc[14], acc[15]});
    const float sm = (s4[0] + s4[1]) + (s4[2] + s4[3]);
    const float lmin = fminf(fminf(lm[0][0], lm[1][0]), fminf(lm[2][0], l0)), lmax = fmaxf(fmaxf(lm[0][0], lm[1][0]), fmaxf(lm[2][0], l0));
    const bool ok = lmin >= 0x1p-40f && lmax <= 0x1p40f && fabsf(sm) <= 3.0e38f;
    if (__builtin_expect(!__all(ok), 0)) {
      const bf16x8 q0 = asb(bldh(rq, l16, qo[0])), q1 = asb(bldh(rq, l16 + 1024, qo[0]));
      float m = -INFINITY; acc = zero16();
#pragma unroll 1
      for (int kt = 0; kt < 64; ++kt) {
        f32x16 s = MFMA(asb(bldh(rkv, l16, kt << 12)), q0, zero16()); s = MFMA(asb(bldh(rkv, l16 + 1024, kt << 12)), q1, s);
        const float mn = fmaxf(m, swapmax(max16(s)));
        const float al = EX2(m - mn); m = mn;
#pragma unroll
        for (int i = 0; i < 16; ++i) { acc[i] *= al; s[i] = EX2(s[i] - mn); }
        acc = MFMA(asb(bldh(rkv, l16 + 2048, kt << 12)), asb(pk8(s, 0)), acc); acc = MFMA(asb(bldh(rkv, l16 + 3072, kt << 12)), asb(pk8(s, 1)), acc);
      }
      put(acc);
    }
  }

  asm volatile("s_waitcnt vmcnt(0)"
               : "+{v[128:131]}"(KA0), "+{v[132:135]}"(KA1), "+{v[136:139]}"(VA0), "+{v[140:143]}"(VA1), "+{v[144:147]}"(KB0), "+{v[148:151]}"(KB1), "+{v[152:155]}"(VB0), "+{v[156:159]}"(VB1),
                 "+{v[160:163]}"(KC0), "+{v[164:167]}"(KC1), "+{v[168:171]}"(VC0), "+{v[172:175]}"(VC1) :: "memory");
  }

  u64 tg;
  {
    int old;
    asm volatile("s_mov_b64 exec, 1\n\tds_add_rtn_u32 %0, %2, %3\n\ts_mov_b64 exec, -1\n\tds_read_b64 %1, %2 offset:%4\n\ts_waitcnt lgkmcnt(0)"
                 : "=&v"(old), "=&v"(tg) : "v"((unsigned)(uintptr_t)(lds + K2_SW + 4 * SW_OD)), "v"(1), "i"(4 * (SW_TAG - SW_OD)) : "memory");
    if (lane == 0 && old == 3) __hip_atomic_store((gu64*)pop, tg, RLX_AGENT);
  }

  if (nt3 == 7) return;
  {
    f32x16 acc0, acc1;
    sw_wait<0>(lds, SW_W3, 3);
    sw_wait<0>(lds, SW_OD, 4);

    if (wave == 0) {
      const int o1[1] = {lane & 15};
      __builtin_amdgcn_s_sleep(4);
      flag_wait(pwp - WS_SY, o1, tg); sw_add(lds, SW_PO);
    } else sw_wait<1>(lds, SW_PO, 1);

    p3_tokens_b(tk, tokp, lane);
    p3_mma_b(tk, lds, kh, lane, acc0, acc1);
    const f32x16 keep = p3_give(lds + 16384, wave, kh, lane, acc0, acc1); sw_add(lds, SW_RB);

    sw_wait<0>(lds, SW_RB, 4);
    p3_stage(lds + 16384, lds, wave, lane, keep);
    sw_add(lds, SW_RB); sw_wait<0>(lds, SW_RB, 8);
    p3_rows<false>(lds, wave, lane, tt3, out, 896 + nt3 * 128);
  }
}

extern "C" void kernel_launch(void* const* d_in, const int* in_sizes, int n_in, void* d_out, int out_size, void* d_ws, size_t ws_size, hipStream_t stream) {
  fused<<<256, 512, 0, stream>>>((const u16*)d_in[0], (const u16*)d_in[1], (const u16*)d_in[2], (const u16*)d_in[3], (const u16*)d_in[4], (const u16*)d_in[5], (u16*)d_out, (char*)d_ws);
}
```
